```python
import torch

def _softmax(x, axis=-1):
    return torch.softmax(x, dim=axis)
def _gelu(x):
    return torch.nn.functional.gelu(x, approximate="tanh")
def _silu(x):
    return torch.nn.functional.silu(x)
def _sigmoid(x):
    return torch.sigmoid(x)
def _layernorm(x, axis=-1, eps=1e-5):
    m = x.mean(dim=axis, keepdim=True)
    v = x.var(dim=axis, keepdim=True, unbiased=False)
    return (x - m) / torch.sqrt(v + eps)
def _scatter_add(tgt, idx, src, axis=0):
    return tgt.clone().index_add_(axis, idx, src)
def _segment_sum(src, seg, n):
    out = torch.zeros((n,) + src.shape[1:], dtype=src.dtype)
    return out.index_add_(0, seg, src)
def _pad(x, pw):
    flat = tuple(v for pair in reversed(pw) for v in pair)
    return torch.nn.functional.pad(x, flat)

kernel_name = 'standalone__batchnorm__sampling'

def setup_inputs():

    _w = (lambda a: (lambda t: t.to(torch.bfloat16) if t.is_floating_point() else t)(torch.as_tensor(a)))
    B, S, C = 1024, 4, 512
    K = 3
    return {
        "x": _w((lambda *s: torch.randn(*s, dtype=torch.bfloat16) * 0.05)(B, S, C)),
        "w": _w((lambda *s: torch.randn(*s, dtype=torch.bfloat16) * 0.05)(K, C, C) * (1.0 / (K * C) ** 0.5)),
        "mean": _w((lambda *s: torch.randn(*s, dtype=torch.bfloat16) * 0.05)(C) * 0.1),
        "var": _w(torch.abs((lambda *s: torch.randn(*s, dtype=torch.bfloat16) * 0.05)(C)) + 0.5),
        "g": _w((lambda *s: torch.randn(*s, dtype=torch.bfloat16) * 0.05)(C) + 1.0),
        "b": _w((lambda *s: torch.randn(*s, dtype=torch.bfloat16) * 0.05)(C) * 0.1),
    }

def reference(x, w, mean, var, g, b):
    K = 3
    P = K // 2
    S = x.shape[1]
    xp = _pad(x, ((0, 0), (P, P), (0, 0)))
    y = sum(torch.einsum('bsc,cd->bsd', xp[:, k:k + S, :], w[k]) for k in range(K))
    inv_std = 1.0 / torch.sqrt(var + 1e-5)
    y = (y - mean) * (g * inv_std) + b
    return _silu(y)
```

```cpp
#include <hip/hip_runtime.h>
#include <hip/hip_bf16.h>
#include <stdint.h>

typedef __attribute__((ext_vector_type(8))) __bf16 bf16x8;
typedef __attribute__((ext_vector_type(4))) short s16x4;
typedef __attribute__((ext_vector_type(8))) short s16x8;
typedef __attribute__((ext_vector_type(16))) float f32x16;
typedef __attribute__((ext_vector_type(4))) unsigned u32x4;
typedef __attribute__((ext_vector_type(2))) unsigned u32x2;
typedef __attribute__((address_space(3))) char lds_char;
typedef __attribute__((address_space(3))) s16x4 lds_s16x4;
typedef __attribute__((address_space(3))) s16x8 lds_s16x8;

constexpr int STAGE_B = 40960;
constexpr int XB = 16384;
constexpr int NSLOT = 4;
constexpr int NSTAGE = 8;

#define WAIT_V(n) asm volatile("s_waitcnt vmcnt(%0)" ::"n"(n) : "memory")

__device__ __forceinline__ float bf2f(unsigned short v) { return __uint_as_float(((unsigned)v) << 16); }
__device__ __forceinline__ unsigned pack_bf16(float lo, float hi) {
  unsigned r;
  asm("v_cvt_pk_bf16_f32 %0, %1, %2" : "=v"(r) : "v"(lo), "v"(hi));
  return r;
}
__device__ __forceinline__ float silu(float y) {
  return y * __builtin_amdgcn_rcpf(1.0f + __expf(-y));
}

__global__ __launch_bounds__(256, 1) void conv_bn_silu(
    const char* __restrict__ x, const char* __restrict__ w, const unsigned short* __restrict__ mean,
    const unsigned short* __restrict__ var, const unsigned short* __restrict__ g,
    const unsigned short* __restrict__ bb, char* __restrict__ out) {
  __shared__ __attribute__((aligned(1024))) char shm[NSLOT * STAGE_B];
  const int tid = threadIdx.x;
  const int wid = __builtin_amdgcn_readfirstlane(tid >> 6);
  const int lane = tid & 63;
  const int xcd = blockIdx.x & 7, idx = blockIdx.x >> 3;
  const int mt = (xcd >> 1) * 8 + (idx & 7);
  const int nt = (xcd & 1) * 4 + (idx >> 3);
  const int b0 = mt * 32, n0 = nt * 64;

  const int lr = lane >> 3, ls = lane & 7;
  const char* xsrc0 = x + (size_t)(b0 * 4 + wid) * 1024 + lr * 4096 + ((ls ^ (lane >> 4)) * 16);
  const char* xsrc1 = x + (size_t)(b0 * 4 + wid) * 1024 + lr * 4096 + ((ls ^ (4 + (lane >> 4))) * 16);
  const char* wsrc = w + n0 * 2 + lr * 1024 + ((ls ^ (((lane >> 4) & 1) << 2)) * 16);

  auto stage = [&](int st) {
    char* slot = shm + (st % NSLOT) * STAGE_B;
#pragma unroll
    for (int j = 0; j < 4; ++j) {
      const char* src = ((j & 1) ? xsrc1 : xsrc0) + j * 8 * 4096 + st * 128;
      __builtin_amdgcn_global_load_lds((const unsigned*)src, (unsigned*)(slot + (wid * 4 + j) * 1024), 16, 0, 0);
    }
#pragma unroll
    for (int j = 0; j < 6; ++j) {
      const int r = wid * 48 + j * 8;
      const int k = r >> 6, cc = r & 63;
      const char* src = wsrc + (size_t)(k * 512 + st * 64 + cc) * 1024;
      __builtin_amdgcn_global_load_lds((const unsigned*)src, (unsigned*)(slot + XB + (wid * 6 + j) * 1024), 16, 0, 0);
    }
  };

  const int nh = wid & 1, sp = wid >> 1;
  const int h = lane >> 5, bl = lane & 31;

  float scale[16], shift[16];
  {
    const int dbase = n0 + 32 * nh + 4 * h;
#pragma unroll
    for (int gq = 0; gq < 4; ++gq) {
      const u32x2 m2 = *(const u32x2*)(mean + dbase + 8 * gq);
      const u32x2 v2 = *(const u32x2*)(var + dbase + 8 * gq);
      const u32x2 g2 = *(const u32x2*)(g + dbase + 8 * gq);
      const u32x2 b2 = *(const u32x2*)(bb + dbase + 8 * gq);
#pragma unroll
      for (int i = 0; i < 4; ++i) {
        const unsigned sh = (i & 1) * 16;
        const float mf = bf2f((unsigned short)(m2[i >> 1] >> sh));
        const float vf = bf2f((unsigned short)(v2[i >> 1] >> sh));
        const float gf = bf2f((unsigned short)(g2[i >> 1] >> sh));
        const float bf = bf2f((unsigned short)(b2[i >> 1] >> sh));
        const float sc = gf * __builtin_amdgcn_rsqf(vf + 1e-5f);
        scale[gq * 4 + i] = sc;
        shift[gq * 4 + i] = bf - mf * sc;
      }
    }
  }

  stage(0); stage(1); stage(2); stage(3);

  int xoff[4];
#pragma unroll
  for (int t = 0; t < 4; ++t) xoff[t] = bl * 128 + (((2 * t + h) ^ ((bl >> 1) & 7)) * 16);
  const int blk = (lane >> 4) & 1, q = (lane & 15) >> 2, p = lane & 3;
  const int woff = XB + (8 * h + q) * 128 + (((4 * nh + 2 * blk + (p >> 1)) ^ (((q >> 1) & 1) << 2)) * 16) + 8 * (p & 1);
  const int k0 = sp ? 2 : 0, k2 = sp ? 0 : 2;
  const int s0 = sp ? 3 : 0, s1 = sp ? 2 : 1, s2 = sp ? 1 : 2;

  f32x16 accA = {}, accB = {};

#pragma unroll
  for (int st = 0; st < NSTAGE; ++st) {
    if (st == 0) WAIT_V(30);
    else if (st <= 5) WAIT_V(20);
    else if (st == 6) WAIT_V(10);
    else WAIT_V(0);
    __builtin_amdgcn_s_barrier();
    asm volatile("" ::: "memory");
    if (st >= 1 && st + 3 < NSTAGE) stage(st + 3);
    lds_char* slot = (lds_char*)shm + (st % NSLOT) * STAGE_B;
#pragma unroll
    for (int t = 0; t < 4; ++t) {
      const s16x8 y0 = *(lds_s16x8*)(slot + s0 * 4096 + xoff[t]);
      const s16x8 y1 = *(lds_s16x8*)(slot + s1 * 4096 + xoff[t]);
      const s16x8 y2 = *(lds_s16x8*)(slot + s2 * 4096 + xoff[t]);
      s16x8 u[3];
#pragma unroll
      for (int i = 0; i < 3; ++i) {
        const int k = (i == 0) ? k0 : (i == 1) ? 1 : k2;
        const s16x4 lo = __builtin_amdgcn_ds_read_tr16_b64_v4i16((lds_s16x4*)(slot + woff + (k * 64 + 16 * t) * 128));
        const s16x4 hi = __builtin_amdgcn_ds_read_tr16_b64_v4i16((lds_s16x4*)(slot + woff + (k * 64 + 16 * t + 4) * 128));
        u[i] = __builtin_shufflevector(lo, hi, 0, 1, 2, 3, 4, 5, 6, 7);
      }
      accA = __builtin_amdgcn_mfma_f32_32x32x16_bf16(__builtin_bit_cast(bf16x8, u[0]), __builtin_bit_cast(bf16x8, y0), accA, 0, 0, 0);
      accB = __builtin_amdgcn_mfma_f32_32x32x16_bf16(__builtin_bit_cast(bf16x8, u[1]), __builtin_bit_cast(bf16x8, y0), accB, 0, 0, 0);
      accA = __builtin_amdgcn_mfma_f32_32x32x16_bf16(__builtin_bit_cast(bf16x8, u[1]), __builtin_bit_cast(bf16x8, y1), accA, 0, 0, 0);
      accB = __builtin_amdgcn_mfma_f32_32x32x16_bf16(__builtin_bit_cast(bf16x8, u[2]), __builtin_bit_cast(bf16x8, y1), accB, 0, 0, 0);
      accA = __builtin_amdgcn_mfma_f32_32x32x16_bf16(__builtin_bit_cast(bf16x8, u[2]), __builtin_bit_cast(bf16x8, y2), accA, 0, 0, 0);
    }
    asm volatile("s_waitcnt lgkmcnt(0)" ::: "memory");
  }

  const int sA = sp ? 2 : 1, sB = sp ? 3 : 0;
  char* orow = out + (size_t)(b0 + bl) * 4096 + (n0 + 32 * nh + 8 * h) * 2;
#pragma unroll
  for (int a = 0; a < 2; ++a) {
    const f32x16 acc = a ? accB : accA;
    const int s = a ? sB : sA;
    unsigned pk[4][2];
#pragma unroll
    for (int gq = 0; gq < 4; ++gq)
#pragma unroll
      for (int i = 0; i < 2; ++i) {
        const float v0 = silu(acc[gq * 4 + 2 * i] * scale[gq * 4 + 2 * i] + shift[gq * 4 + 2 * i]);
        const float v1 = silu(acc[gq * 4 + 2 * i + 1] * scale[gq * 4 + 2 * i + 1] + shift[gq * 4 + 2 * i + 1]);
        pk[gq][i] = pack_bf16(v0, v1);
      }
#pragma unroll
    for (int j = 0; j < 2; ++j) {
      const auto r0 = __builtin_amdgcn_permlane32_swap(pk[2 * j][0], pk[2 * j + 1][0], false, false);
      const auto r1 = __builtin_amdgcn_permlane32_swap(pk[2 * j][1], pk[2 * j + 1][1], false, false);
      u32x4 v;
      v[0] = r0[0]; v[1] = r1[0]; v[2] = r0[1]; v[3] = r1[1];
      *(u32x4*)(orow + s * 1024 + j * 32) = v;
    }
  }
}

extern "C" void kernel_launch(void* const* d_in, const int* in_sizes, int n_in, void* d_out, int out_size,
                              void* d_ws, size_t ws_size, hipStream_t stream) {
  conv_bn_silu<<<256, 256, 0, stream>>>((const char*)d_in[0], (const char*)d_in[1], (const unsigned short*)d_in[2],
                                        (const unsigned short*)d_in[3], (const unsigned short*)d_in[4],
                                        (const unsigned short*)d_in[5], (char*)d_out);
}
```

```cpp
#include <hip/hip_runtime.h>
#include <hip/hip_bf16.h>
#include <stdint.h>

typedef __attribute__((ext_vector_type(8))) __bf16 bf16x8;
typedef __attribute__((ext_vector_type(4))) short s16x4;
typedef __attribute__((ext_vector_type(8))) short s16x8;
typedef __attribute__((ext_vector_type(16))) float f32x16;
typedef __attribute__((ext_vector_type(4))) unsigned u32x4;
typedef __attribute__((ext_vector_type(2))) unsigned u32x2;
typedef __attribute__((address_space(3))) char lds_char;
typedef __attribute__((address_space(3))) void lds_void;

constexpr int STAGE_B = 40960;
constexpr int XB = 16384;
constexpr int NSLOT = 4;
constexpr int NSTAGE = 8;
constexpr int SRD3 = 0x00020000;

#define WAIT_V(n) asm volatile("s_waitcnt vmcnt(%0)" ::"n"(n) : "memory")
#define BARRIER() do { __builtin_amdgcn_s_barrier(); asm volatile("" ::: "memory"); } while (0)

#define ST(i)

__device__ __forceinline__ float bf2f(unsigned short v) { return __uint_as_float(((unsigned)v) << 16); }
__device__ __forceinline__ unsigned pack_bf16(float lo, float hi) {
  unsigned r;
  asm("v_cvt_pk_bf16_f32 %0, %1, %2" : "=v"(r) : "v"(lo), "v"(hi));
  return r;
}
__device__ __forceinline__ float silu(float y) { return y * __builtin_amdgcn_rcpf(1.0f + __expf(-y)); }

struct Frags { s16x8 y0, y1, y2; s16x4 u0l, u0h, u1l, u1h, u2l, u2h; };

#define RD128(dst, addr, off) asm volatile("ds_read_b128 %0, %1 offset:%2" : "=v"(dst) : "v"(addr), "n"(off))
#define RDTR(dst, addr, off) asm volatile("ds_read_b64_tr_b16 %0, %1 offset:%2" : "=v"(dst) : "v"(addr), "n"(off))
#define WAITF(F) asm volatile("s_waitcnt lgkmcnt(0)" : "+v"(F.y0), "+v"(F.y1), "+v"(F.y2), "+v"(F.u0l), "+v"(F.u0h), "+v"(F.u1l), "+v"(F.u1h), "+v"(F.u2l), "+v"(F.u2h))
#define CAT(l, h) __builtin_bit_cast(bf16x8, __builtin_shufflevector(l, h, 0, 1, 2, 3, 4, 5, 6, 7))
#define BC(v) __builtin_bit_cast(bf16x8, v)
#define MFMA(a, b, c) asm volatile("v_mfma_f32_32x32x16_bf16 %0, %1, %2, %0" : "+v"(c) : "v"(a), "v"(b))
#define SB() __builtin_amdgcn_sched_barrier(0)

#define KSTEP(C, N, T)                                                   \
  do {                                                                   \
    MFMA(CAT(C.u0l, C.u0h), BC(C.y0), accA); SB();                       \
    RD128(N.y0, ax0[T], 0); RDTR(N.u0l, aw0, (T) * 2048); RDTR(N.u0h, aw0, (T) * 2048 + 512); SB(); \
    MFMA(CAT(C.u1l, C.u1h), BC(C.y0), accB); SB();                       \
    RDTR(N.u1l, aw1, (T) * 2048); RDTR(N.u1h, aw1, (T) * 2048 + 512); RD128(N.y1, ax1[T], 0); SB(); \
    MFMA(CAT(C.u1l, C.u1h), BC(C.y1), accA); SB();                       \
    RDTR(N.u2l, aw2, (T) * 2048); RDTR(N.u2h, aw2, (T) * 2048 + 512); RD128(N.y2, ax2[T], 0); SB(); \
    MFMA(CAT(C.u2l, C.u2h), BC(C.y1), accB); SB();                       \
    MFMA(CAT(C.u2l, C.u2h), BC(C.y2), accA); SB();                       \
  } while (0)

__global__ __launch_bounds__(512, 2) void conv_bn_silu(
    const char* __restrict__ x, const char* __restrict__ w, const unsigned short* __restrict__ mean,
    const unsigned short* __restrict__ var, const unsigned short* __restrict__ g,
    const unsigned short* __restrict__ bb, char* __restrict__ out
    ) {
  __shared__ __attribute__((aligned(1024))) char shm[NSLOT * STAGE_B];
  const int tid = threadIdx.x;
  const int wid = __builtin_amdgcn_readfirstlane(tid >> 6);
  const int lane = tid & 63;
  const int xcd = blockIdx.x & 7, idx = blockIdx.x >> 3;
  const int mt = (xcd >> 1) * 8 + (idx & 7);
  const int nt = (xcd & 1) * 4 + (idx >> 3);
  const int b0 = mt * 32, n0 = nt * 64;
  ST(0);

  if (wid >= 4) {
    const int lw = wid - 4;
    const __amdgpu_buffer_rsrc_t rx = __builtin_amdgcn_make_buffer_rsrc((void*)x, 0, 1024 * 4 * 512 * 2, SRD3);
    const __amdgpu_buffer_rsrc_t rw = __builtin_amdgcn_make_buffer_rsrc((void*)w, 0, 3 * 512 * 512 * 2, SRD3);
    const int lr = lane >> 3, ls = lane & 7;
    const int vx0 = lr * 4096 + ((ls ^ (lane >> 4)) * 16);
    const int vx1 = lr * 4096 + ((ls ^ (4 + (lane >> 4))) * 16);
    const int vw = lr * 1024 + ((ls ^ (((lane >> 4) & 1) << 2)) * 16);
    const int sx = (b0 * 4 + lw) * 1024;
    auto stage = [&](int st, int slotoff) {
      lds_char* slot = (lds_char*)shm + slotoff;
#pragma unroll
      for (int j = 0; j < 4; ++j)
        __builtin_amdgcn_raw_ptr_buffer_load_lds(rx, (lds_void*)(slot + (lw * 4 + j) * 1024), 16, (j & 1) ? vx1 : vx0,
                                                 sx + j * 8 * 4096 + st * 128, 0, 0);
#pragma unroll
      for (int j = 0; j < 6; ++j) {
        const int r = lw * 48 + j * 8;
        const int k = r >> 6, cc = r & 63;
        __builtin_amdgcn_raw_ptr_buffer_load_lds(rw, (lds_void*)(slot + XB + (lw * 6 + j) * 1024), 16, vw,
                                                 (k * 512 + st * 64 + cc) * 1024 + n0 * 2, 0, 0);
      }
    };
#pragma unroll 1
    for (int st = 0; st < 4; ++st) stage(st, st * STAGE_B);
    ST(1);
    WAIT_V(30);
    ST(2);
    BARRIER();
    int slotoff = 0;
#pragma unroll 1
    for (int k = 1; k <= 4; ++k) {
      WAIT_V(20);
      BARRIER();
      stage(k + 3, slotoff);
      slotoff += STAGE_B;
    }
    ST(3);
    WAIT_V(20);
    BARRIER();
    WAIT_V(10);
    BARRIER();
    WAIT_V(0);
    ST(4);
    BARRIER();
    BARRIER();
    return;
  }

  const int nh = wid & 1, sp = wid >> 1;
  const int h = lane >> 5, bl = lane & 31;

  float scale[16], shift[16];
  {
    const int dbase = n0 + 32 * nh + 4 * h;
#pragma unroll
    for (int gq = 0; gq < 4; ++gq) {
      const u32x2 m2 = *(const u32x2*)(mean + dbase + 8 * gq);
      const u32x2 v2 = *(const u32x2*)(var + dbase + 8 * gq);
      const u32x2 g2 = *(const u32x2*)(g + dbase + 8 * gq);
      const u32x2 b2 = *(const u32x2*)(bb + dbase + 8 * gq);
#pragma unroll
      for (int i = 0; i < 4; ++i) {
        const unsigned sh = (i & 1) * 16;
        const float mf = bf2f((unsigned short)(m2[i >> 1] >> sh));
        const float vf = bf2f((unsigned short)(v2[i >> 1] >> sh));
        const float gf = bf2f((unsigned short)(g2[i >> 1] >> sh));
        const float bf = bf2f((unsigned short)(b2[i >> 1] >> sh));
        const float sc = gf * __builtin_amdgcn_rsqf(vf + 1e-5f);
        scale[gq * 4 + i] = sc;
        shift[gq * 4 + i] = bf - mf * sc;
      }
    }
#pragma unroll
    for (int i = 0; i < 16; ++i) asm volatile("" : "+v"(scale[i]), "+v"(shift[i]));
  }

  const unsigned lbase = (unsigned)(size_t)(lds_char*)shm;
  const int k0 = sp ? 2 : 0, k2 = sp ? 0 : 2;
  const int s0 = sp ? 3 : 0, s1 = sp ? 2 : 1, s2 = sp ? 1 : 2;
  unsigned ax0[4], ax1[4], ax2[4];
#pragma unroll
  for (int t = 0; t < 4; ++t) {
    const unsigned o = lbase + bl * 128 + (((2 * t + h) ^ ((bl >> 1) & 7)) * 16);
    ax0[t] = o + s0 * 4096; ax1[t] = o + s1 * 4096; ax2[t] = o + s2 * 4096;
  }
  const int blk = (lane >> 4) & 1, q = (lane & 15) >> 2, p = lane & 3;
  const unsigned woff = lbase + XB + (8 * h + q) * 128 + (((4 * nh + 2 * blk + (p >> 1)) ^ (((q >> 1) & 1) << 2)) * 16) + 8 * (p & 1);
  unsigned aw0 = woff + k0 * 8192, aw1 = woff + 8192, aw2 = woff + k2 * 8192;

  f32x16 accA = {}, accB = {};
  Frags F0, F1;
  ST(1);
  BARRIER();
  ST(2);
  RD128(F0.y0, ax0[0], 0); RDTR(F0.u0l, aw0, 0); RDTR(F0.u0h, aw0, 512);
  RDTR(F0.u1l, aw1, 0); RDTR(F0.u1h, aw1, 512); RD128(F0.y1, ax1[0], 0);
  RDTR(F0.u2l, aw2, 0); RDTR(F0.u2h, aw2, 512); RD128(F0.y2, ax2[0], 0);

  int slot = 0;
#pragma unroll 1
  for (int st = 0; st < NSTAGE; ++st) {
    WAITF(F0); SB();
    KSTEP(F0, F1, 1);
    WAITF(F1); SB();
    KSTEP(F1, F0, 2);
    WAITF(F0); SB();
    KSTEP(F0, F1, 3);
    WAITF(F1); SB();
    BARRIER();
    const int delta = (slot == 3) ? -3 * STAGE_B : STAGE_B;
    slot = (slot + 1) & 3;
#pragma unroll
    for (int t = 0; t < 4; ++t) { ax0[t] += delta; ax1[t] += delta; ax2[t] += delta; }
    aw0 += delta; aw1 += delta; aw2 += delta;
    SB();
    KSTEP(F1, F0, 0);
  }
  WAITF(F0);
  asm volatile("s_nop 7\n\ts_nop 7" : "+v"(accA), "+v"(accB));
  ST(3);

  const int sA = sp ? 2 : 1, sB = sp ? 3 : 0;
  char* orow = out + (size_t)(b0 + bl) * 4096 + (n0 + 32 * nh + 8 * h) * 2;
#pragma unroll
  for (int a = 0; a < 2; ++a) {
    const f32x16 acc = a ? accB : accA;
    const int s = a ? sB : sA;
    unsigned pk[4][2];
#pragma unroll
    for (int gq = 0; gq < 4; ++gq)
#pragma unroll
      for (int i = 0; i < 2; ++i) {
        const float v0 = silu(acc[gq * 4 + 2 * i] * scale[gq * 4 + 2 * i] + shift[gq * 4 + 2 * i]);
        const float v1 = silu(acc[gq * 4 + 2 * i + 1] * scale[gq * 4 + 2 * i + 1] + shift[gq * 4 + 2 * i + 1]);
        pk[gq][i] = pack_bf16(v0, v1);
      }
#pragma unroll
    for (int j = 0; j < 2; ++j) {
      const auto r0 = __builtin_amdgcn_permlane32_swap(pk[2 * j][0], pk[2 * j + 1][0], false, false);
      const auto r1 = __builtin_amdgcn_permlane32_swap(pk[2 * j][1], pk[2 * j + 1][1], false, false);
      u32x4 v;
      v[0] = r0[0]; v[1] = r1[0]; v[2] = r0[1]; v[3] = r1[1];
      *(u32x4*)(orow + s * 1024 + j * 32) = v;
    }
  }
  ST(4);
}

extern "C" void kernel_launch(void* const* d_in, const int* in_sizes, int n_in, void* d_out, int out_size,
                              void* d_ws, size_t ws_size, hipStream_t stream) {
  conv_bn_silu<<<256, 512, 0, stream>>>((const char*)d_in[0], (const char*)d_in[1], (const unsigned short*)d_in[2],
                                        (const unsigned short*)d_in[3], (const unsigned short*)d_in[4],
                                        (const unsigned short*)d_in[5], (char*)d_out
                                        );
}
```

```cpp
#include <hip/hip_runtime.h>
#include <hip/hip_bf16.h>
#include <stdint.h>

typedef __attribute__((ext_vector_type(8))) __bf16 bf16x8;
typedef __attribute__((ext_vector_type(4))) short s16x4;
typedef __attribute__((ext_vector_type(8))) short s16x8;
typedef __attribute__((ext_vector_type(16))) float f32x16;
typedef __attribute__((ext_vector_type(4))) unsigned u32x4;
typedef __attribute__((ext_vector_type(2))) unsigned u32x2;
typedef __attribute__((address_space(3))) char lds_char;
typedef __attribute__((address_space(3))) void lds_void;

constexpr int STAGE_B = 40960;
constexpr int XB = 16384;
constexpr int NSLOT = 4;
constexpr int NSTAGE = 8;
constexpr int SRD3 = 0x00020000;

#define WAIT_V(n) asm volatile("s_waitcnt vmcnt(%0)" ::"n"(n) : "memory")
#define BARRIER() do { __builtin_amdgcn_s_barrier(); asm volatile("" ::: "memory"); } while (0)

#define ST(i)

__device__ __forceinline__ float bf2f(unsigned short v) { return __uint_as_float(((unsigned)v) << 16); }
__device__ __forceinline__ unsigned pack_bf16(float lo, float hi) {
  unsigned r;
  asm("v_cvt_pk_bf16_f32 %0, %1, %2" : "=v"(r) : "v"(lo), "v"(hi));
  return r;
}
__device__ __forceinline__ float silu(float y) { return y * __builtin_amdgcn_rcpf(1.0f + __expf(-y)); }

struct Frags { s16x8 y0, y1, y2; s16x4 u0l, u0h, u1l, u1h, u2l, u2h; };

#define RD128(dst, addr, off) asm volatile("ds_read_b128 %0, %1 offset:%2" : "=v"(dst) : "v"(addr), "n"(off))
#define RDTR(dst, addr, off) asm volatile("ds_read_b64_tr_b16 %0, %1 offset:%2" : "=v"(dst) : "v"(addr), "n"(off))
#define WAITF(F) asm volatile("s_waitcnt lgkmcnt(0)" : "+v"(F.y0), "+v"(F.y1), "+v"(F.y2), "+v"(F.u0l), "+v"(F.u0h), "+v"(F.u1l), "+v"(F.u1h), "+v"(F.u2l), "+v"(F.u2h))
#define CAT(l, h) __builtin_bit_cast(bf16x8, __builtin_shufflevector(l, h, 0, 1, 2, 3, 4, 5, 6, 7))
#define BC(v) __builtin_bit_cast(bf16x8, v)
#define MFMA(a, b, c) asm volatile("v_mfma_f32_32x32x16_bf16 %0, %1, %2, %0" : "+v"(c) : "v"(a), "v"(b))
#define SB() __builtin_amdgcn_sched_barrier(0)

#define KSTEP(C, N, T)                                                   \
  do {                                                                   \
    MFMA(CAT(C.u0l, C.u0h), BC(C.y0), accA); SB();                       \
    RD128(N.y0, ax0[T], 0); RDTR(N.u0l, aw0, (T) * 2048); RDTR(N.u0h, aw0, (T) * 2048 + 512); SB(); \
    MFMA(CAT(C.u1l, C.u1h), BC(C.y0), accB); SB();                       \
    RDTR(N.u1l, aw1, (T) * 2048); RDTR(N.u1h, aw1, (T) * 2048 + 512); RD128(N.y1, ax1[T], 0); SB(); \
    MFMA(CAT(C.u1l, C.u1h), BC(C.y1), accA); SB();                       \
    RDTR(N.u2l, aw2, (T) * 2048); RDTR(N.u2h, aw2, (T) * 2048 + 512); RD128(N.y2, ax2[T], 0); SB(); \
    MFMA(CAT(C.u2l, C.u2h), BC(C.y1), accB); SB();                       \
    MFMA(CAT(C.u2l, C.u2h), BC(C.y2), accA); SB();                       \
  } while (0)

__global__ __launch_bounds__(512, 2) void conv_bn_silu(
    const char* __restrict__ x, const char* __restrict__ w, const unsigned short* __restrict__ mean,
    const unsigned short* __restrict__ var, const unsigned short* __restrict__ g,
    const unsigned short* __restrict__ bb, char* __restrict__ out
    ) {
  __shared__ __attribute__((aligned(1024))) char shm[NSLOT * STAGE_B];
  const int tid = threadIdx.x;
  const int wid = __builtin_amdgcn_readfirstlane(tid >> 6);
  const int lane = tid & 63;
  const int xcd = blockIdx.x & 7, idx = blockIdx.x >> 3;
  const int mt = (xcd >> 1) * 8 + (idx & 7);
  const int nt = (xcd & 1) * 4 + (idx >> 3);
  const int b0 = mt * 32, n0 = nt * 64;
  ST(0);
  const unsigned long long T0 = __builtin_amdgcn_s_memrealtime();

  if (wid >= 4) {
    const int lw = wid - 4;
    const __amdgpu_buffer_rsrc_t rx = __builtin_amdgcn_make_buffer_rsrc((void*)x, 0, 1024 * 4 * 512 * 2, SRD3);
    const __amdgpu_buffer_rsrc_t rw = __builtin_amdgcn_make_buffer_rsrc((void*)w, 0, 3 * 512 * 512 * 2, SRD3);
    const int lr = lane >> 3, ls = lane & 7;
    const int vx0 = lr * 4096 + ((ls ^ (lane >> 4)) * 16);
    const int vx1 = lr * 4096 + ((ls ^ (4 + (lane >> 4))) * 16);
    const int vw = lr * 1024 + ((ls ^ (((lane >> 4) & 1) << 2)) * 16);
    const int sx = (b0 * 4 + lw) * 1024;
    auto stage = [&](int st, int slotoff) {
      lds_char* slot = (lds_char*)shm + slotoff;
#pragma unroll
      for (int j = 0; j < 4; ++j)
        __builtin_amdgcn_raw_ptr_buffer_load_lds(rx, (lds_void*)(slot + (lw * 4 + j) * 1024), 16, (j & 1) ? vx1 : vx0,
                                                 sx + j * 8 * 4096 + st * 128, 0, 0);
#pragma unroll
      for (int j = 0; j < 6; ++j) {
        const int r = lw * 48 + j * 8;
        const int k = r >> 6, cc = r & 63;
        __builtin_amdgcn_raw_ptr_buffer_load_lds(rw, (lds_void*)(slot + XB + (lw * 6 + j) * 1024), 16, vw,
                                                 (k * 512 + st * 64 + cc) * 1024 + n0 * 2, 0, 0);
      }
    };
#pragma unroll 1
    for (int st = 0; st < 4; ++st) stage(st, st * STAGE_B);
    ST(1);
    WAIT_V(30);
    ST(2);
    BARRIER();
    int slotoff = 0;
#pragma unroll 1
    for (int k = 1; k <= 4; ++k) {
      WAIT_V(20);
      BARRIER();
      stage(k + 3, slotoff);
      slotoff += STAGE_B;
    }
    ST(3);
    WAIT_V(20);
    BARRIER();
    WAIT_V(10);
    BARRIER();
    WAIT_V(0);
    ST(4);
    BARRIER();
    BARRIER();
    return;
  }

  const int nh = wid & 1, sp = wid >> 1;
  const int h = lane >> 5, bl = lane & 31;

  float scale[16], shift[16];
  {
    const int dbase = n0 + 32 * nh + 4 * h;
#pragma unroll
    for (int gq = 0; gq < 4; ++gq) {
      const u32x2 m2 = *(const u32x2*)(mean + dbase + 8 * gq);
      const u32x2 v2 = *(const u32x2*)(var + dbase + 8 * gq);
      const u32x2 g2 = *(const u32x2*)(g + dbase + 8 * gq);
      const u32x2 b2 = *(const u32x2*)(bb + dbase + 8 * gq);
#pragma unroll
      for (int i = 0; i < 4; ++i) {
        const unsigned sh = (i & 1) * 16;
        const float mf = bf2f((unsigned short)(m2[i >> 1] >> sh));
        const float vf = bf2f((unsigned short)(v2[i >> 1] >> sh));
        const float gf = bf2f((unsigned short)(g2[i >> 1] >> sh));
        const float bf = bf2f((unsigned short)(b2[i >> 1] >> sh));
        const float sc = gf * __builtin_amdgcn_rsqf(vf + 1e-5f);
        scale[gq * 4 + i] = sc;
        shift[gq * 4 + i] = bf - mf * sc;
      }
    }
#pragma unroll
    for (int i = 0; i < 16; ++i) asm volatile("" : "+v"(scale[i]), "+v"(shift[i]));
  }

  const unsigned lbase = (unsigned)(size_t)(lds_char*)shm;
  const int k0 = sp ? 2 : 0, k2 = sp ? 0 : 2;
  const int s0 = sp ? 3 : 0, s1 = sp ? 2 : 1, s2 = sp ? 1 : 2;
  unsigned ax0[4], ax1[4], ax2[4];
#pragma unroll
  for (int t = 0; t < 4; ++t) {
    const unsigned o = lbase + bl * 128 + (((2 * t + h) ^ ((bl >> 1) & 7)) * 16);
    ax0[t] = o + s0 * 4096; ax1[t] = o + s1 * 4096; ax2[t] = o + s2 * 4096;
  }
  const int blk = (lane >> 4) & 1, q = (lane & 15) >> 2, p = lane & 3;
  const unsigned woff = lbase + XB + (8 * h + q) * 128 + (((4 * nh + 2 * blk + (p >> 1)) ^ (((q >> 1) & 1) << 2)) * 16) + 8 * (p & 1);
  unsigned aw0 = woff + k0 * 8192, aw1 = woff + 8192, aw2 = woff + k2 * 8192;

  f32x16 accA = {}, accB = {};
  Frags F0, F1;
  ST(1);
  BARRIER();
  const unsigned long long T1 = __builtin_amdgcn_s_memrealtime();
  ST(2);
  RD128(F0.y0, ax0[0], 0); RDTR(F0.u0l, aw0, 0); RDTR(F0.u0h, aw0, 512);
  RDTR(F0.u1l, aw1, 0); RDTR(F0.u1h, aw1, 512); RD128(F0.y1, ax1[0], 0);
  RDTR(F0.u2l, aw2, 0); RDTR(F0.u2h, aw2, 512); RD128(F0.y2, ax2[0], 0);

  int slot = 0;
#pragma unroll 1
  for (int st = 0; st < NSTAGE; ++st) {
    WAITF(F0); SB();
    KSTEP(F0, F1, 1);
    WAITF(F1); SB();
    KSTEP(F1, F0, 2);
    WAITF(F0); SB();
    KSTEP(F0, F1, 3);
    WAITF(F1); SB();
    BARRIER();
    const int delta = (slot == 3) ? -3 * STAGE_B : STAGE_B;
    slot = (slot + 1) & 3;
#pragma unroll
    for (int t = 0; t < 4; ++t) { ax0[t] += delta; ax1[t] += delta; ax2[t] += delta; }
    aw0 += delta; aw1 += delta; aw2 += delta;
    SB();
    KSTEP(F1, F0, 0);
  }
  WAITF(F0);
  asm volatile("s_nop 7\n\ts_nop 7" : "+v"(accA), "+v"(accB));
  const unsigned long long T2 = __builtin_amdgcn_s_memrealtime();
  ST(3);

  const int sA = sp ? 2 : 1, sB = sp ? 3 : 0;
  char* orow = out + (size_t)(b0 + bl) * 4096 + (n0 + 32 * nh + 8 * h) * 2;
#pragma unroll
  for (int a = 0; a < 2; ++a) {
    const f32x16 acc = a ? accB : accA;
    const int s = a ? sB : sA;
    unsigned pk[4][2];
#pragma unroll
    for (int gq = 0; gq < 4; ++gq)
#pragma unroll
      for (int i = 0; i < 2; ++i) {
        const float v0 = silu(acc[gq * 4 + 2 * i] * scale[gq * 4 + 2 * i] + shift[gq * 4 + 2 * i]);
        const float v1 = silu(acc[gq * 4 + 2 * i + 1] * scale[gq * 4 + 2 * i + 1] + shift[gq * 4 + 2 * i + 1]);
        pk[gq][i] = pack_bf16(v0, v1);
      }
#pragma unroll
    for (int j = 0; j < 2; ++j) {
      const auto r0 = __builtin_amdgcn_permlane32_swap(pk[2 * j][0], pk[2 * j + 1][0], false, false);
      const auto r1 = __builtin_amdgcn_permlane32_swap(pk[2 * j][1], pk[2 * j + 1][1], false, false);
      u32x4 v;
      v[0] = r0[0]; v[1] = r1[0]; v[2] = r0[1]; v[3] = r1[1];
      *(u32x4*)(orow + s * 1024 + j * 32) = v;
    }
  }
  ST(4);
  {
    asm volatile("s_waitcnt vmcnt(0)" ::: "memory");
    const unsigned long long T3 = __builtin_amdgcn_s_memrealtime();
    const unsigned long long d = 4ull * (0ull * (T1 - T0 + T2));
    while (__builtin_amdgcn_s_memrealtime() - T3 < d) {}
  }
}

extern "C" void kernel_launch(void* const* d_in, const int* in_sizes, int n_in, void* d_out, int out_size,
                              void* d_ws, size_t ws_size, hipStream_t stream) {
  conv_bn_silu<<<256, 512, 0, stream>>>((const char*)d_in[0], (const char*)d_in[1], (const unsigned short*)d_in[2],
                                        (const unsigned short*)d_in[3], (const unsigned short*)d_in[4],
                                        (const unsigned short*)d_in[5], (char*)d_out
                                        );
}
```

```cpp
#include <hip/hip_runtime.h>
#include <hip/hip_bf16.h>
#include <stdint.h>

typedef __attribute__((ext_vector_type(8))) __bf16 bf16x8;
typedef __attribute__((ext_vector_type(4))) short s16x4;
typedef __attribute__((ext_vector_type(8))) short s16x8;
typedef __attribute__((ext_vector_type(16))) float f32x16;
typedef __attribute__((ext_vector_type(4))) unsigned u32x4;
typedef __attribute__((ext_vector_type(2))) unsigned u32x2;
typedef __attribute__((address_space(3))) char lds_char;
typedef __attribute__((address_space(3))) void lds_void;

constexpr int STAGE_B = 40960;
constexpr int XB = 16384;
constexpr int NSLOT = 4;
constexpr int NSTAGE = 8;
constexpr int SRD3 = 0x00020000;

#define WAIT_V(n) asm volatile("s_waitcnt vmcnt(%0)" ::"n"(n) : "memory")
#define BARRIER() do { __builtin_amdgcn_s_barrier(); asm volatile("" ::: "memory"); } while (0)

#define ST(i)

__device__ __forceinline__ float bf2f(unsigned short v) { return __uint_as_float(((unsigned)v) << 16); }
__device__ __forceinline__ unsigned pack_bf16(float lo, float hi) {
  unsigned r;
  asm("v_cvt_pk_bf16_f32 %0, %1, %2" : "=v"(r) : "v"(lo), "v"(hi));
  return r;
}
__device__ __forceinline__ float silu(float y) { return y * __builtin_amdgcn_rcpf(1.0f + __expf(-y)); }

struct Frags { s16x8 y0, y1, y2; s16x4 u0l, u0h, u1l, u1h, u2l, u2h; };

#define RD128(dst, addr, off) asm volatile("ds_read_b128 %0, %1 offset:%2" : "=v"(dst) : "v"(addr), "n"(off))
#define RDTR(dst, addr, off) asm volatile("ds_read_b64_tr_b16 %0, %1 offset:%2" : "=v"(dst) : "v"(addr), "n"(off))
#define WAITF(F) asm volatile("s_waitcnt lgkmcnt(0)" : "+v"(F.y0), "+v"(F.y1), "+v"(F.y2), "+v"(F.u0l), "+v"(F.u0h), "+v"(F.u1l), "+v"(F.u1h), "+v"(F.u2l), "+v"(F.u2h))
#define CAT(l, h) __builtin_bit_cast(bf16x8, __builtin_shufflevector(l, h, 0, 1, 2, 3, 4, 5, 6, 7))
#define BC(v) __builtin_bit_cast(bf16x8, v)
#define MFMA(a, b, c) asm volatile("v_mfma_f32_32x32x16_bf16 %0, %1, %2, %0" : "+v"(c) : "v"(a), "v"(b))
#define SB() __builtin_amdgcn_sched_barrier(0)

#define KSTEP(C, N, T)                                                   \
  do {                                                                   \
    MFMA(CAT(C.u0l, C.u0h), BC(C.y0), accA); SB();                       \
    RD128(N.y0, ax0[T], 0); RDTR(N.u0l, aw0, (T) * 2048); RDTR(N.u0h, aw0, (T) * 2048 + 512); SB(); \
    MFMA(CAT(C.u1l, C.u1h), BC(C.y0), accB); SB();                       \
    RDTR(N.u1l, aw1, (T) * 2048); RDTR(N.u1h, aw1, (T) * 2048 + 512); RD128(N.y1, ax1[T], 0); SB(); \
    MFMA(CAT(C.u1l, C.u1h), BC(C.y1), accA); SB();                       \
    RDTR(N.u2l, aw2, (T) * 2048); RDTR(N.u2h, aw2, (T) * 2048 + 512); RD128(N.y2, ax2[T], 0); SB(); \
    MFMA(CAT(C.u2l, C.u2h), BC(C.y1), accB); SB();                       \
    MFMA(CAT(C.u2l, C.u2h), BC(C.y2), accA); SB();                       \
  } while (0)

__global__ __launch_bounds__(512, 2) void conv_bn_silu(
    const char* __restrict__ x, const char* __restrict__ w, const unsigned short* __restrict__ mean,
    const unsigned short* __restrict__ var, const unsigned short* __restrict__ g,
    const unsigned short* __restrict__ bb, char* __restrict__ out
    ) {
  __shared__ __attribute__((aligned(1024))) char shm[NSLOT * STAGE_B];
  const int tid = threadIdx.x;
  const int wid = __builtin_amdgcn_readfirstlane(tid >> 6);
  const int lane = tid & 63;
  const int xcd = blockIdx.x & 7, idx = blockIdx.x >> 3;
  const int mt = (xcd >> 1) * 8 + (idx & 7);
  const int nt = (xcd & 1) * 4 + (idx >> 3);
  const int b0 = mt * 32, n0 = nt * 64;
  ST(0);

  if (wid >= 4) {
    const int lw = wid - 4;
    const __amdgpu_buffer_rsrc_t rx = __builtin_amdgcn_make_buffer_rsrc((void*)x, 0, 1024 * 4 * 512 * 2, SRD3);
    const __amdgpu_buffer_rsrc_t rw = __builtin_amdgcn_make_buffer_rsrc((void*)w, 0, 3 * 512 * 512 * 2, SRD3);
    const int lr = lane >> 3, ls = lane & 7;
    const int vx0 = lr * 4096 + ((ls ^ (lane >> 4)) * 16);
    const int vx1 = lr * 4096 + ((ls ^ (4 + (lane >> 4))) * 16);
    const int vw = lr * 1024 + ((ls ^ (((lane >> 4) & 1) << 2)) * 16);
    const int sx = (b0 * 4 + lw) * 1024;
    auto stage = [&](int st, int slotoff) {
      lds_char* slot = (lds_char*)shm + slotoff;
#pragma unroll
      for (int j = 0; j < 4; ++j)
        __builtin_amdgcn_raw_ptr_buffer_load_lds(rx, (lds_void*)(slot + (lw * 4 + j) * 1024), 16, (j & 1) ? vx1 : vx0,
                                                 sx + j * 8 * 4096 + st * 128, 0, 0);
#pragma unroll
      for (int j = 0; j < 6; ++j) {
        const int r = lw * 48 + j * 8;
        const int k = r >> 6, cc = r & 63;
        __builtin_amdgcn_raw_ptr_buffer_load_lds(rw, (lds_void*)(slot + XB + (lw * 6 + j) * 1024), 16, vw,
                                                 (k * 512 + st * 64 + cc) * 1024 + n0 * 2, 0, 0);
      }
    };
#pragma unroll 1
    for (int st = 0; st < 4; ++st) stage(st, st * STAGE_B);
    ST(1);
    WAIT_V(30);
    ST(2);
    BARRIER();
    int slotoff = 0;
#pragma unroll 1
    for (int k = 1; k <= 4; ++k) {
      WAIT_V(20);
      BARRIER();
      stage(k + 3, slotoff);
      slotoff += STAGE_B;
    }
    ST(3);
    WAIT_V(20);
    BARRIER();
    WAIT_V(10);
    BARRIER();
    WAIT_V(0);
    ST(4);
    BARRIER();
    BARRIER();
    return;
  }

  const int nh = wid & 1, sp = wid >> 1;
  const int h = lane >> 5, bl = lane & 31;

  float scale[16], shift[16];
  {
    const int dbase = n0 + 32 * nh + 4 * h;
#pragma unroll
    for (int gq = 0; gq < 4; ++gq) {
      const u32x2 m2 = *(const u32x2*)(mean + dbase + 8 * gq);
      const u32x2 v2 = *(const u32x2*)(var + dbase + 8 * gq);
      const u32x2 g2 = *(const u32x2*)(g + dbase + 8 * gq);
      const u32x2 b2 = *(const u32x2*)(bb + dbase + 8 * gq);
#pragma unroll
      for (int i = 0; i < 4; ++i) {
        const unsigned sh = (i & 1) * 16;
        const float mf = bf2f((unsigned short)(m2[i >> 1] >> sh));
        const float vf = bf2f((unsigned short)(v2[i >> 1] >> sh));
        const float gf = bf2f((unsigned short)(g2[i >> 1] >> sh));
        const float bf = bf2f((unsigned short)(b2[i >> 1] >> sh));
        const float sc = gf * __builtin_amdgcn_rsqf(vf + 1e-5f);
        scale[gq * 4 + i] = sc;
        shift[gq * 4 + i] = bf - mf * sc;
      }
    }
#pragma unroll
    for (int i = 0; i < 16; ++i) asm volatile("" : "+v"(scale[i]), "+v"(shift[i]));
  }

  const unsigned lbase = (unsigned)(size_t)(lds_char*)shm;
  const int k0 = sp ? 2 : 0, k2 = sp ? 0 : 2;
  const int s0 = sp ? 3 : 0, s1 = sp ? 2 : 1, s2 = sp ? 1 : 2;
  unsigned ax0[4], ax1[4], ax2[4];
#pragma unroll
  for (int t = 0; t < 4; ++t) {
    const unsigned o = lbase + bl * 128 + (((2 * t + h) ^ ((bl >> 1) & 7)) * 16);
    ax0[t] = o + s0 * 4096; ax1[t] = o + s1 * 4096; ax2[t] = o + s2 * 4096;
  }
  const int blk = (lane >> 4) & 1, q = (lane & 15) >> 2, p = lane & 3;
  const unsigned woff = lbase + XB + (8 * h + q) * 128 + (((4 * nh + 2 * blk + (p >> 1)) ^ (((q >> 1) & 1) << 2)) * 16) + 8 * (p & 1);
  unsigned aw0 = woff + k0 * 8192, aw1 = woff + 8192, aw2 = woff + k2 * 8192;

  f32x16 accA = {}, accB = {};
  Frags F0, F1;
  ST(1);
  BARRIER();
  ST(2);
  RD128(F0.y0, ax0[0], 0); RDTR(F0.u0l, aw0, 0); RDTR(F0.u0h, aw0, 512);
  RDTR(F0.u1l, aw1, 0); RDTR(F0.u1h, aw1, 512); RD128(F0.y1, ax1[0], 0);
  RDTR(F0.u2l, aw2, 0); RDTR(F0.u2h, aw2, 512); RD128(F0.y2, ax2[0], 0);

  int slot = 0;
#pragma unroll 1
  for (int st = 0; st < NSTAGE; ++st) {
    WAITF(F0); SB();
    KSTEP(F0, F1, 1);
    WAITF(F1); SB();
    KSTEP(F1, F0, 2);
    WAITF(F0); SB();
    KSTEP(F0, F1, 3);
    WAITF(F1); SB();
    BARRIER();
    const int delta = (slot == 3) ? -3 * STAGE_B : STAGE_B;
    slot = (slot + 1) & 3;
#pragma unroll
    for (int t = 0; t < 4; ++t) { ax0[t] += delta; ax1[t] += delta; ax2[t] += delta; }
    aw0 += delta; aw1 += delta; aw2 += delta;
    SB();
    KSTEP(F1, F0, 0);
  }
  WAITF(F0);
  asm volatile("s_nop 7\n\ts_nop 7" : "+v"(accA), "+v"(accB));
  ST(3);

  const int sA = sp ? 2 : 1, sB = sp ? 3 : 0;
  const __amdgpu_buffer_rsrc_t ro = __builtin_amdgcn_make_buffer_rsrc((void*)out, 0, 1024 * 4 * 512 * 2, SRD3);
  const int orow = (b0 + bl) * 4096 + (n0 + 32 * nh + 8 * h) * 2;
#pragma unroll
  for (int a = 0; a < 2; ++a) {
    const f32x16 acc = a ? accB : accA;
    const int s = a ? sB : sA;
    unsigned pk[4][2];
#pragma unroll
    for (int gq = 0; gq < 4; ++gq)
#pragma unroll
      for (int i = 0; i < 2; ++i) {
        const float v0 = silu(acc[gq * 4 + 2 * i] * scale[gq * 4 + 2 * i] + shift[gq * 4 + 2 * i]);
        const float v1 = silu(acc[gq * 4 + 2 * i + 1] * scale[gq * 4 + 2 * i + 1] + shift[gq * 4 + 2 * i + 1]);
        pk[gq][i] = pack_bf16(v0, v1);
      }
#pragma unroll
    for (int j = 0; j < 2; ++j) {
      const auto r0 = __builtin_amdgcn_permlane32_swap(pk[2 * j][0], pk[2 * j + 1][0], false, false);
      const auto r1 = __builtin_amdgcn_permlane32_swap(pk[2 * j][1], pk[2 * j + 1][1], false, false);
      u32x4 v;
      v[0] = r0[0]; v[1] = r1[0]; v[2] = r0[1]; v[3] = r1[1];
      __builtin_amdgcn_raw_buffer_store_b128(v, ro, orow + j * 32, s * 1024, 2);
    }
  }
  ST(4);
}

extern "C" void kernel_launch(void* const* d_in, const int* in_sizes, int n_in, void* d_out, int out_size,
                              void* d_ws, size_t ws_size, hipStream_t stream) {
  conv_bn_silu<<<256, 512, 0, stream>>>((const char*)d_in[0], (const char*)d_in[1], (const unsigned short*)d_in[2],
                                        (const unsigned short*)d_in[3], (const unsigned short*)d_in[4],
                                        (const unsigned short*)d_in[5], (char*)d_out
                                        );
}
```

```cpp
#include <hip/hip_runtime.h>
#include <hip/hip_bf16.h>
#include <stdint.h>

typedef __attribute__((ext_vector_type(8))) __bf16 bf16x8;
typedef __attribute__((ext_vector_type(4))) short s16x4;
typedef __attribute__((ext_vector_type(8))) short s16x8;
typedef __attribute__((ext_vector_type(16))) float f32x16;
typedef __attribute__((ext_vector_type(4))) unsigned u32x4;
typedef __attribute__((ext_vector_type(2))) unsigned u32x2;
typedef __attribute__((address_space(3))) char lds_char;
typedef __attribute__((address_space(3))) void lds_void;

constexpr int STAGE_B = 40960;
constexpr int XB = 16384;
constexpr int NSLOT = 4;
constexpr int NSTAGE = 8;
constexpr int SRD3 = 0x00020000;

#define WAIT_V(n) asm volatile("s_waitcnt vmcnt(%0)" ::"n"(n) : "memory")
#define BARRIER() do { __builtin_amdgcn_s_barrier(); asm volatile("" ::: "memory"); } while (0)

#define ST(i)

__device__ __forceinline__ float bf2f(unsigned short v) { return __uint_as_float(((unsigned)v) << 16); }
__device__ __forceinline__ unsigned pack_bf16(float lo, float hi) {
  unsigned r;
  asm("v_cvt_pk_bf16_f32 %0, %1, %2" : "=v"(r) : "v"(lo), "v"(hi));
  return r;
}
__device__ __forceinline__ float silu(float y) { return y * __builtin_amdgcn_rcpf(1.0f + __expf(-y)); }

struct Frags { s16x8 y0, y1, y2; s16x4 u0l, u0h, u1l, u1h, u2l, u2h; };

#define RD128(dst, addr, off) asm volatile("ds_read_b128 %0, %1 offset:%2" : "=v"(dst) : "v"(addr), "n"(off))
#define RDTR(dst, addr, off) asm volatile("ds_read_b64_tr_b16 %0, %1 offset:%2" : "=v"(dst) : "v"(addr), "n"(off))
#define WAITF(F) asm volatile("s_waitcnt lgkmcnt(0)" : "+v"(F.y0), "+v"(F.y1), "+v"(F.y2), "+v"(F.u0l), "+v"(F.u0h), "+v"(F.u1l), "+v"(F.u1h), "+v"(F.u2l), "+v"(F.u2h))
#define CAT(l, h) __builtin_bit_cast(bf16x8, __builtin_shufflevector(l, h, 0, 1, 2, 3, 4, 5, 6, 7))
#define BC(v) __builtin_bit_cast(bf16x8, v)
#define MFMA(a, b, c) asm volatile("v_mfma_f32_32x32x16_bf16 %0, %1, %2, %0" : "+v"(c) : "v"(a), "v"(b))
#define SB() __builtin_amdgcn_sched_barrier(0)

#define KSTEP(C, N, T)                                                   \
  do {                                                                   \
    MFMA(CAT(C.u0l, C.u0h), BC(C.y0), accA); SB();                       \
    RD128(N.y0, ax0[T], 0); RDTR(N.u0l, aw0, (T) * 2048); RDTR(N.u0h, aw0, (T) * 2048 + 512); SB(); \
    MFMA(CAT(C.u1l, C.u1h), BC(C.y0), accB); SB();                       \
    RDTR(N.u1l, aw1, (T) * 2048); RDTR(N.u1h, aw1, (T) * 2048 + 512); RD128(N.y1, ax1[T], 0); SB(); \
    MFMA(CAT(C.u1l, C.u1h), BC(C.y1), accA); SB();                       \
    RDTR(N.u2l, aw2, (T) * 2048); RDTR(N.u2h, aw2, (T) * 2048 + 512); RD128(N.y2, ax2[T], 0); SB(); \
    MFMA(CAT(C.u2l, C.u2h), BC(C.y1), accB); SB();                       \
    MFMA(CAT(C.u2l, C.u2h), BC(C.y2), accA); SB();                       \
  } while (0)

__global__ __launch_bounds__(512, 2) void conv_bn_silu(
    const char* __restrict__ x, const char* __restrict__ w, const unsigned short* __restrict__ mean,
    const unsigned short* __restrict__ var, const unsigned short* __restrict__ g,
    const unsigned short* __restrict__ bb, char* __restrict__ out
    , unsigned long long* __restrict__ wsp
    ) {
  __shared__ __attribute__((aligned(1024))) char shm[NSLOT * STAGE_B];
  const int tid = threadIdx.x;
  const int wid = __builtin_amdgcn_readfirstlane(tid >> 6);
  const int lane = tid & 63;
  const int xcd = blockIdx.x & 7, idx = blockIdx.x >> 3;
  const int mt = (xcd >> 1) * 8 + (idx & 7);
  const int nt = (xcd & 1) * 4 + (idx >> 3);
  const int b0 = mt * 32, n0 = nt * 64;
  const unsigned long long T0 = __builtin_amdgcn_s_memrealtime();

  if (wid >= 4) {
    const int lw = wid - 4;
    const __amdgpu_buffer_rsrc_t rx = __builtin_amdgcn_make_buffer_rsrc((void*)x, 0, 1024 * 4 * 512 * 2, SRD3);
    const __amdgpu_buffer_rsrc_t rw = __builtin_amdgcn_make_buffer_rsrc((void*)w, 0, 3 * 512 * 512 * 2, SRD3);
    const int lr = lane >> 3, ls = lane & 7;
    const int vx0 = lr * 4096 + ((ls ^ (lane >> 4)) * 16);
    const int vx1 = lr * 4096 + ((ls ^ (4 + (lane >> 4))) * 16);
    const int vw = lr * 1024 + ((ls ^ (((lane >> 4) & 1) << 2)) * 16);
    const int sx = (b0 * 4 + lw) * 1024;
    auto stage = [&](int st, int slotoff) {
      lds_char* slot = (lds_char*)shm + slotoff;
#pragma unroll
      for (int j = 0; j < 4; ++j)
        __builtin_amdgcn_raw_ptr_buffer_load_lds(rx, (lds_void*)(slot + (lw * 4 + j) * 1024), 16, (j & 1) ? vx1 : vx0,
                                                 sx + j * 8 * 4096 + st * 128, 0, 0);
#pragma unroll
      for (int j = 0; j < 6; ++j) {
        const int r = lw * 48 + j * 8;
        const int k = r >> 6, cc = r & 63;
        __builtin_amdgcn_raw_ptr_buffer_load_lds(rw, (lds_void*)(slot + XB + (lw * 6 + j) * 1024), 16, vw,
                                                 (k * 512 + st * 64 + cc) * 1024 + n0 * 2, 0, 0);
      }
    };
#pragma unroll 1
    for (int st = 0; st < 4; ++st) stage(st, st * STAGE_B);
    ST(1);
    WAIT_V(30);
    ST(2);
    BARRIER();
    int slotoff = 0;
#pragma unroll 1
    for (int k = 1; k <= 4; ++k) {
      WAIT_V(20);
      BARRIER();
      stage(k + 3, slotoff);
      slotoff += STAGE_B;
    }
    ST(3);
    WAIT_V(20);
    BARRIER();
    WAIT_V(10);
    BARRIER();
    WAIT_V(0);
    ST(4);
    BARRIER();
    BARRIER();
    return;
  }

  const int nh = wid & 1, sp = wid >> 1;
  const int h = lane >> 5, bl = lane & 31;

  float scale[16], shift[16];
  {
    const int dbase = n0 + 32 * nh + 4 * h;
#pragma unroll
    for (int gq = 0; gq < 4; ++gq) {
      const u32x2 m2 = *(const u32x2*)(mean + dbase + 8 * gq);
      const u32x2 v2 = *(const u32x2*)(var + dbase + 8 * gq);
      const u32x2 g2 = *(const u32x2*)(g + dbase + 8 * gq);
      const u32x2 b2 = *(const u32x2*)(bb + dbase + 8 * gq);
#pragma unroll
      for (int i = 0; i < 4; ++i) {
        const unsigned sh = (i & 1) * 16;
        const float mf = bf2f((unsigned short)(m2[i >> 1] >> sh));
        const float vf = bf2f((unsigned short)(v2[i >> 1] >> sh));
        const float gf = bf2f((unsigned short)(g2[i >> 1] >> sh));
        const float bf = bf2f((unsigned short)(b2[i >> 1] >> sh));
        const float sc = gf * __builtin_amdgcn_rsqf(vf + 1e-5f);
        scale[gq * 4 + i] = sc;
        shift[gq * 4 + i] = bf - mf * sc;
      }
    }
#pragma unroll
    for (int i = 0; i < 16; ++i) asm volatile("" : "+v"(scale[i]), "+v"(shift[i]));
  }

  const unsigned lbase = (unsigned)(size_t)(lds_char*)shm;
  const int k0 = sp ? 2 : 0, k2 = sp ? 0 : 2;
  const int s0 = sp ? 3 : 0, s1 = sp ? 2 : 1, s2 = sp ? 1 : 2;
  unsigned ax0[4], ax1[4], ax2[4];
#pragma unroll
  for (int t = 0; t < 4; ++t) {
    const unsigned o = lbase + bl * 128 + (((2 * t + h) ^ ((bl >> 1) & 7)) * 16);
    ax0[t] = o + s0 * 4096; ax1[t] = o + s1 * 4096; ax2[t] = o + s2 * 4096;
  }
  const int blk = (lane >> 4) & 1, q = (lane & 15) >> 2, p = lane & 3;
  const unsigned woff = lbase + XB + (8 * h + q) * 128 + (((4 * nh + 2 * blk + (p >> 1)) ^ (((q >> 1) & 1) << 2)) * 16) + 8 * (p & 1);
  unsigned aw0 = woff + k0 * 8192, aw1 = woff + 8192, aw2 = woff + k2 * 8192;

  f32x16 accA = {}, accB = {};
  Frags F0, F1;
  ST(1);
  BARRIER();
  ST(2);
  RD128(F0.y0, ax0[0], 0); RDTR(F0.u0l, aw0, 0); RDTR(F0.u0h, aw0, 512);
  RDTR(F0.u1l, aw1, 0); RDTR(F0.u1h, aw1, 512); RD128(F0.y1, ax1[0], 0);
  RDTR(F0.u2l, aw2, 0); RDTR(F0.u2h, aw2, 512); RD128(F0.y2, ax2[0], 0);

  int slot = 0;
#pragma unroll 1
  for (int st = 0; st < NSTAGE; ++st) {
    WAITF(F0); SB();
    KSTEP(F0, F1, 1);
    WAITF(F1); SB();
    KSTEP(F1, F0, 2);
    WAITF(F0); SB();
    KSTEP(F0, F1, 3);
    WAITF(F1); SB();
    BARRIER();
    const int delta = (slot == 3) ? -3 * STAGE_B : STAGE_B;
    slot = (slot + 1) & 3;
#pragma unroll
    for (int t = 0; t < 4; ++t) { ax0[t] += delta; ax1[t] += delta; ax2[t] += delta; }
    aw0 += delta; aw1 += delta; aw2 += delta;
    SB();
    KSTEP(F1, F0, 0);
  }
  WAITF(F0);
  asm volatile("s_nop 7\n\ts_nop 7" : "+v"(accA), "+v"(accB));
  ST(3);

  const int sA = sp ? 2 : 1, sB = sp ? 3 : 0;
  char* orow = out + (size_t)(b0 + bl) * 4096 + (n0 + 32 * nh + 8 * h) * 2;
#pragma unroll
  for (int a = 0; a < 2; ++a) {
    const f32x16 acc = a ? accB : accA;
    const int s = a ? sB : sA;
    unsigned pk[4][2];
#pragma unroll
    for (int gq = 0; gq < 4; ++gq)
#pragma unroll
      for (int i = 0; i < 2; ++i) {
        const float v0 = silu(acc[gq * 4 + 2 * i] * scale[gq * 4 + 2 * i] + shift[gq * 4 + 2 * i]);
        const float v1 = silu(acc[gq * 4 + 2 * i + 1] * scale[gq * 4 + 2 * i + 1] + shift[gq * 4 + 2 * i + 1]);
        pk[gq][i] = pack_bf16(v0, v1);
      }
#pragma unroll
    for (int j = 0; j < 2; ++j) {
      const auto r0 = __builtin_amdgcn_permlane32_swap(pk[2 * j][0], pk[2 * j + 1][0], false, false);
      const auto r1 = __builtin_amdgcn_permlane32_swap(pk[2 * j][1], pk[2 * j + 1][1], false, false);
      u32x4 v;
      v[0] = r0[0]; v[1] = r1[0]; v[2] = r0[1]; v[3] = r1[1];
      *(u32x4*)(orow + s * 1024 + j * 32) = v;
    }
  }
  ST(4);
  {
    asm volatile("s_waitcnt vmcnt(0)" ::: "memory");
    const unsigned long long tk = wsp[0];
    const unsigned long long T3 = __builtin_amdgcn_s_memrealtime();
    const unsigned long long d = 0ull * (T0 - tk);
    while (__builtin_amdgcn_s_memrealtime() - T3 < d) {}
  }
}

__global__ void tick_k(unsigned long long* p) { if (threadIdx.x == 0) p[0] = __builtin_amdgcn_s_memrealtime(); }
__global__ void post_k(unsigned long long* p, int mult) {
  const unsigned long long t = __builtin_amdgcn_s_memrealtime();
  unsigned long long m = p[512 + threadIdx.x];
  __shared__ unsigned long long sm[256];
  sm[threadIdx.x] = m; __syncthreads();
  for (int i = 0; i < 256; ++i) m = sm[i] > m ? sm[i] : m;
  const unsigned long long d = (unsigned long long)mult * (t - m);
  const unsigned long long t1 = __builtin_amdgcn_s_memrealtime();
  while (__builtin_amdgcn_s_memrealtime() - t1 < d) {}
}

extern "C" void kernel_launch(void* const* d_in, const int* in_sizes, int n_in, void* d_out, int out_size,
                              void* d_ws, size_t ws_size, hipStream_t stream) {
  tick_k<<<1, 64, 0, stream>>>((unsigned long long*)d_ws);
  conv_bn_silu<<<256, 512, 0, stream>>>((const char*)d_in[0], (const char*)d_in[1], (const unsigned short*)d_in[2],
                                        (const unsigned short*)d_in[3], (const unsigned short*)d_in[4],
                                        (const unsigned short*)d_in[5], (char*)d_out
                                        , (unsigned long long*)d_ws
                                        );
}
```

```cpp
#include <hip/hip_runtime.h>
#include <hip/hip_bf16.h>
#include <stdint.h>

typedef __attribute__((ext_vector_type(8))) __bf16 bf16x8;
typedef __attribute__((ext_vector_type(4))) short s16x4;
typedef __attribute__((ext_vector_type(8))) short s16x8;
typedef __attribute__((ext_vector_type(16))) float f32x16;
typedef __attribute__((ext_vector_type(4))) float f32x4;
typedef __attribute__((ext_vector_type(2))) float f32x2;
typedef __attribute__((ext_vector_type(4))) unsigned u32x4;
typedef __attribute__((address_space(3))) char lds_char;
typedef __attribute__((address_space(3))) void lds_void;

#define ROT 4
#define LDAUX 0
#define STAUX 2

constexpr int STAGE_B = 40960;
constexpr int XB = 16384;
constexpr int NSLOT = 4;
constexpr int PARAM_OFF = XB;
constexpr int SRD3 = 0x00020000;

#define WAIT_V(n) asm volatile("s_waitcnt vmcnt(%0)" ::"n"(n) : "memory")
#define WAIT_L0() asm volatile("s_waitcnt lgkmcnt(0)" ::: "memory")
#define BARRIER() do { asm volatile("" ::: "memory"); __builtin_amdgcn_s_barrier(); asm volatile("" ::: "memory"); } while (0)

#define ST(i)
#define STAMP_ARG
#define STAMP_PASS

__device__ __forceinline__ float bf2f(unsigned short v) { return __uint_as_float(((unsigned)v) << 16); }
__device__ __forceinline__ unsigned pack_bf16(float lo, float hi) {
  unsigned r;
  asm("v_cvt_pk_bf16_f32 %0, %1, %2" : "=v"(r) : "v"(lo), "v"(hi));
  return r;
}

struct Frags { s16x8 y0, y1, y2; s16x4 u0l, u0h, u1l, u1h, u2l, u2h; };

#define RD128(dst, addr, off) asm volatile("ds_read_b128 %0, %1 offset:%2" : "=v"(dst) : "v"(addr), "n"(off))
#define RDTR(dst, addr, off) asm volatile("ds_read_b64_tr_b16 %0, %1 offset:%2" : "=v"(dst) : "v"(addr), "n"(off))
#define WL1(n, a) asm volatile("s_waitcnt lgkmcnt(%1)" : "+v"(a) : "n"(n))
#define WL2(n, a, b) asm volatile("s_waitcnt lgkmcnt(%2)" : "+v"(a), "+v"(b) : "n"(n))
#define WL3(n, a, b, c) asm volatile("s_waitcnt lgkmcnt(%3)" : "+v"(a), "+v"(b), "+v"(c) : "n"(n))
#define CAT(l, h) __builtin_bit_cast(bf16x8, __builtin_shufflevector(l, h, 0, 1, 2, 3, 4, 5, 6, 7))
#define BC(v) __builtin_bit_cast(bf16x8, v)
#define MFMA(a, b, c) asm volatile("v_mfma_f32_32x32x16_bf16 %0, %1, %2, %0" : "+v"(c) : "v"(a), "v"(b))
#define SB() __builtin_amdgcn_sched_barrier(0)

template <int SP>
struct Cfg {
  static constexpr int S0 = SP ? 3 : 0, S1 = SP ? 2 : 1, S2 = SP ? 1 : 2;
  static constexpr int K0 = SP ? 2 : 0, K1 = 1, K2 = SP ? 0 : 2;
  static constexpr int SA = SP ? 2 : 1, SBo = SP ? 3 : 0;
};

#define WOFF(K, T) (XB + (K) * 8192 + (T) * 2048)
#define KSTEP(C, N, T)                                                                        \
  do {                                                                                        \
    WL3(6, C.u0l, C.u0h, C.y0); SB();                                                         \
    MFMA(CAT(C.u0l, C.u0h), BC(C.y0), accA); SB();                                            \
    if ((T) == 0) { BARRIER(); SB(); }                                                        \
    RDTR(N.u0l, aw, WOFF(G::K0, T)); RDTR(N.u0h, aw, WOFF(G::K0, T) + 512);                   \
    RD128(N.y0, ax[T], G::S0 * 4096); SB();                                                   \
    WL2(7, C.u1l, C.u1h); SB();                                                               \
    MFMA(CAT(C.u1l, C.u1h), BC(C.y0), accB); SB();                                            \
    RDTR(N.u1l, aw, WOFF(G::K1, T)); RDTR(N.u1h, aw, WOFF(G::K1, T) + 512); SB();             \
    WL1(8, C.y1); SB();                                                                       \
    MFMA(CAT(C.u1l, C.u1h), BC(C.y1), accA); SB();                                            \
    RD128(N.y1, ax[T], G::S1 * 4096);                                                         \
    RDTR(N.u2l, aw, WOFF(G::K2, T)); RDTR(N.u2h, aw, WOFF(G::K2, T) + 512); SB();             \
    WL2(9, C.u2l, C.u2h); SB();                                                               \
    MFMA(CAT(C.u2l, C.u2h), BC(C.y1), accB); SB();                                            \
    RD128(N.y2, ax[T], G::S2 * 4096); SB();                                                   \
    WL1(9, C.y2); SB();                                                                       \
    MFMA(CAT(C.u2l, C.u2h), BC(C.y2), accA); SB();                                            \
  } while (0)

__device__ __forceinline__ void epi_half(const f32x16& acc, const f32x4 (&sc)[4], const f32x4 (&sh)[4], unsigned (&pk)[8]) {
#pragma unroll
  for (int i = 0; i < 8; ++i) {
    const f32x2 a = {acc[2 * i], acc[2 * i + 1]};
    const f32x2 s = {sc[i >> 1][(i & 1) * 2], sc[i >> 1][(i & 1) * 2 + 1]};
    const f32x2 f = {sh[i >> 1][(i & 1) * 2], sh[i >> 1][(i & 1) * 2 + 1]};
    const f32x2 y = a * s + f;
    const f32x2 t = y * -1.44269504088896340736f;
    f32x2 e = {__builtin_amdgcn_exp2f(t[0]), __builtin_amdgcn_exp2f(t[1])};
    e = e + 1.0f;
    const f32x2 r = {__builtin_amdgcn_rcpf(e[0]), __builtin_amdgcn_rcpf(e[1])};
    const f32x2 o = y * r;
    pk[i] = pack_bf16(o[0], o[1]);
  }
}

template <int SP>
__device__ __forceinline__ void consume(char* shm, int wid, int lane STAMP_ARG) {
  using G = Cfg<SP>;
  const int nh = wid & 1;
  const int h = lane >> 5, bl = lane & 31;
  const unsigned lbase = (unsigned)(size_t)(lds_char*)shm;
  unsigned ax[4];
#pragma unroll
  for (int t = 0; t < 4; ++t) ax[t] = lbase + bl * 128 + (((2 * t + h) ^ ((bl >> 1) & 7)) * 16);
  const int blk = (lane >> 4) & 1, q = (lane & 15) >> 2, p = lane & 3;
  unsigned aw = lbase + (8 * h + q) * 128 + (((4 * nh + 2 * (p & 1) + blk) ^ (((q >> 1) & 1) << 2)) * 16) + 8 * (p >> 1);
  const unsigned apar = lbase + PARAM_OFF + (32 * nh + 16 * h) * 4;
  const unsigned astg = lbase + bl * 128;
  const unsigned c0 = ((4 * nh + 2 * h) ^ (bl & 7)) * 16, c1 = ((4 * nh + 2 * h + 1) ^ (bl & 7)) * 16;

  f32x16 accA = {}, accB = {};
  Frags F0, F1;
  ST(1);
  BARRIER();
  ST(2);
  RDTR(F0.u0l, aw, WOFF(G::K0, 0)); RDTR(F0.u0h, aw, WOFF(G::K0, 0) + 512); RD128(F0.y0, ax[0], G::S0 * 4096);
  RDTR(F0.u1l, aw, WOFF(G::K1, 0)); RDTR(F0.u1h, aw, WOFF(G::K1, 0) + 512);
  RD128(F0.y1, ax[0], G::S1 * 4096); RDTR(F0.u2l, aw, WOFF(G::K2, 0)); RDTR(F0.u2h, aw, WOFF(G::K2, 0) + 512);
  RD128(F0.y2, ax[0], G::S2 * 4096);

#pragma unroll 1
  for (int st = 0; st < 7; ++st) {
    KSTEP(F0, F1, 1);
    KSTEP(F1, F0, 2);
    KSTEP(F0, F1, 3);
    const int delta = (st == 3) ? -3 * STAGE_B : STAGE_B;
#pragma unroll
    for (int t = 0; t < 4; ++t) ax[t] += delta;
    aw += delta;
    SB();
    KSTEP(F1, F0, 0);
  }
  KSTEP(F0, F1, 1);
  KSTEP(F1, F0, 2);
  KSTEP(F0, F1, 3);

  f32x4 sc[4], sh[4];
  WL3(6, F1.u0l, F1.u0h, F1.y0); SB();
  MFMA(CAT(F1.u0l, F1.u0h), BC(F1.y0), accA); SB();
  RD128(sc[0], apar, 0); RD128(sh[0], apar, 256); SB();
  WL2(6, F1.u1l, F1.u1h); SB();
  MFMA(CAT(F1.u1l, F1.u1h), BC(F1.y0), accB); SB();
  RD128(sc[1], apar, 16); RD128(sh[1], apar, 272); SB();
  WL1(7, F1.y1); SB();
  MFMA(CAT(F1.u1l, F1.u1h), BC(F1.y1), accA); SB();
  RD128(sc[2], apar, 32); RD128(sh[2], apar, 288); SB();
  WL2(7, F1.u2l, F1.u2h); SB();
  MFMA(CAT(F1.u2l, F1.u2h), BC(F1.y1), accB); SB();
  RD128(sc[3], apar, 48); RD128(sh[3], apar, 304); SB();
  WL1(8, F1.y2); SB();
  MFMA(CAT(F1.u2l, F1.u2h), BC(F1.y2), accA); SB();
  asm volatile("s_waitcnt lgkmcnt(0)\n\ts_nop 7"
               : "+v"(sc[0]), "+v"(sc[1]), "+v"(sc[2]), "+v"(sc[3]), "+v"(sh[0]), "+v"(sh[1]), "+v"(sh[2]), "+v"(sh[3]), "+v"(accB));
  SB();
  ST(3);

  unsigned pk[8];
  epi_half(accB, sc, sh, pk);
  {
    const u32x4 v0 = {pk[0], pk[1], pk[2], pk[3]}, v1 = {pk[4], pk[5], pk[6], pk[7]};
    asm volatile("ds_write_b128 %0, %1 offset:%2" ::"v"(astg + c0), "v"(v0), "n"(G::SBo * 4096) : "memory");
    asm volatile("ds_write_b128 %0, %1 offset:%2" ::"v"(astg + c1), "v"(v1), "n"(G::SBo * 4096) : "memory");
  }
  SB();
  asm volatile("s_nop 3" : "+v"(accA));
  WAIT_L0();
  BARRIER();
  SB();
  ST(4);
  epi_half(accA, sc, sh, pk);
  {
    const u32x4 v0 = {pk[0], pk[1], pk[2], pk[3]}, v1 = {pk[4], pk[5], pk[6], pk[7]};
    asm volatile("ds_write_b128 %0, %1 offset:%2" ::"v"(astg + c0), "v"(v0), "n"(G::SA * 4096) : "memory");
    asm volatile("ds_write_b128 %0, %1 offset:%2" ::"v"(astg + c1), "v"(v1), "n"(G::SA * 4096) : "memory");
  }
  WAIT_L0();
  BARRIER();
  ST(5);
}

__global__ __launch_bounds__(512, 2) void conv_bn_silu(
    const char* __restrict__ x, const char* __restrict__ w, const unsigned short* __restrict__ mean,
    const unsigned short* __restrict__ var, const unsigned short* __restrict__ g,
    const unsigned short* __restrict__ bb, char* __restrict__ out STAMP_ARG) {
  __shared__ __attribute__((aligned(1024))) char shm[NSLOT * STAGE_B];
  const int tid = threadIdx.x;
  const int wid = __builtin_amdgcn_readfirstlane(tid >> 6);
  const int lane = tid & 63;
  ST(0);

  if (wid < 4) {
    if (wid & 2) consume<1>(shm, wid, lane STAMP_PASS);
    else consume<0>(shm, wid, lane STAMP_PASS);
    return;
  }

  const int xcd = blockIdx.x & 7, idx = blockIdx.x >> 3;
  const int mt = (xcd >> 1) * 8 + (idx & 7);
  const int nt = (xcd & 1) * 4 + (idx >> 3);
  const int b0 = mt * 32, n0 = nt * 64;
  const int rot = ROT == 8 ? ((idx & 7) + 2 * (idx >> 3)) : ROT == 4 ? 2 * ((idx + (idx >> 3)) & 3) : ROT == 2 ? 4 * ((idx + (idx >> 3)) & 1) : 0;
  const int lw = wid - 4;
  const __amdgpu_buffer_rsrc_t rx = __builtin_amdgcn_make_buffer_rsrc((void*)x, 0, 1024 * 4 * 512 * 2, SRD3);
  const __amdgpu_buffer_rsrc_t rw = __builtin_amdgcn_make_buffer_rsrc((void*)w, 0, 3 * 512 * 512 * 2, SRD3);
  const int lr = lane >> 3, ls = lane & 7;
  const int vx0 = lr * 4096 + ((ls ^ (lane >> 4)) * 16);
  const int vx1 = lr * 4096 + ((ls ^ (4 + (lane >> 4))) * 16);
  const int vw = lr * 1024 + ((ls ^ (((lane >> 4) & 1) << 2)) * 16);
  const int sx = (b0 * 4 + lw) * 1024;
  auto stage = [&](int st, int slotoff) {
    const int cs = (st + rot) & 7;
    lds_char* slot = (lds_char*)shm + slotoff;
#pragma unroll
    for (int j = 0; j < 4; ++j)
      __builtin_amdgcn_raw_ptr_buffer_load_lds(rx, (lds_void*)(slot + (lw * 4 + j) * 1024), 16, (j & 1) ? vx1 : vx0,
                                               sx + j * 8 * 4096 + cs * 128, 0, LDAUX);
#pragma unroll
    for (int j = 0; j < 6; ++j) {
      const int r = lw * 48 + j * 8;
      const int k = r >> 6, cc = r & 63;
      __builtin_amdgcn_raw_ptr_buffer_load_lds(rw, (lds_void*)(slot + XB + (lw * 6 + j) * 1024), 16, vw,
                                               (k * 512 + cs * 64 + cc) * 1024 + n0 * 2, 0, LDAUX);
    }
  };
  const unsigned short pm = mean[n0 + lane], pv = var[n0 + lane], pg = g[n0 + lane], pb = bb[n0 + lane];
  SB();
  stage(0, 0);
  ST(1);
  stage(1, STAGE_B);
  ST(2);
  WAIT_V(10);
  ST(3);
  BARRIER();
  stage(2, 2 * STAGE_B);
  const float scale = bf2f(pg) * __builtin_amdgcn_rsqf(bf2f(pv) + 1e-5f);
  const float shift = bf2f(pb) - bf2f(pm) * scale;
  int slotoff = 3 * STAGE_B;
#pragma unroll 1
  for (int k = 1; k <= 5; ++k) {
    WAIT_V(10);
    BARRIER();
    stage(k + 2, slotoff);
    slotoff = (slotoff == 3 * STAGE_B) ? 0 : slotoff + STAGE_B;
  }
  ST(4);
  WAIT_V(10);
  BARRIER();
  if (lw == 0) {
    const unsigned a = (unsigned)(size_t)(lds_char*)shm + PARAM_OFF + lane * 4;
    asm volatile("ds_write_b32 %0, %1\n\tds_write_b32 %0, %2 offset:256" ::"v"(a), "v"(scale), "v"(shift) : "memory");
  }
  WAIT_V(0);
  WAIT_L0();
  ST(5);
  BARRIER();

  const __amdgpu_buffer_rsrc_t ro = __builtin_amdgcn_make_buffer_rsrc((void*)out, 0, 1024 * 4 * 512 * 2, SRD3);
  const unsigned sbase = (unsigned)(size_t)(lds_char*)shm + (lw * 8 + lr) * 128 + ((ls ^ lr) * 16);
  const int vo = (b0 + lw * 8 + lr) * 4096 + n0 * 2 + ls * 16;
#pragma unroll
  for (int ph = 0; ph < 2; ++ph) {
    BARRIER();
    const int sa = ph ? 1 : 0, sb = ph ? 2 : 3;
    u32x4 va, vb;
    asm volatile("ds_read_b128 %0, %2 offset:%3\n\tds_read_b128 %1, %2 offset:%4\n\ts_waitcnt lgkmcnt(0)"
                 : "=&v"(va), "=&v"(vb) : "v"(sbase), "n"(sa * 4096), "n"(sb * 4096) : "memory");
    __builtin_amdgcn_raw_buffer_store_b128(va, ro, vo, sa * 1024, STAUX);
    __builtin_amdgcn_raw_buffer_store_b128(vb, ro, vo, sb * 1024, STAUX);
    ST(6 + ph);
  }
}

extern "C" void kernel_launch(void* const* d_in, const int* in_sizes, int n_in, void* d_out, int out_size,
                              void* d_ws, size_t ws_size, hipStream_t stream) {
  conv_bn_silu<<<256, 512, 0, stream>>>((const char*)d_in[0], (const char*)d_in[1], (const unsigned short*)d_in[2],
                                        (const unsigned short*)d_in[3], (const unsigned short*)d_in[4],
                                        (const unsigned short*)d_in[5], (char*)d_out
                                        );
}
```

```cpp
#include <hip/hip_runtime.h>
#include <hip/hip_bf16.h>
#include <stdint.h>

typedef __attribute__((ext_vector_type(8))) __bf16 bf16x8;
typedef __attribute__((ext_vector_type(4))) short s16x4;
typedef __attribute__((ext_vector_type(8))) short s16x8;
typedef __attribute__((ext_vector_type(16))) float f32x16;
typedef __attribute__((ext_vector_type(4))) float f32x4;
typedef __attribute__((ext_vector_type(2))) float f32x2;
typedef __attribute__((ext_vector_type(4))) unsigned u32x4;
typedef __attribute__((address_space(3))) char lds_char;
typedef __attribute__((address_space(3))) void lds_void;

#define ROT 4
#define PF 0
#define PFS 0
#define PFD 3
#define LDAUX 1
#define STAUX 2

constexpr int STAGE_B = 40960;
constexpr int XB = 16384;
constexpr int NSLOT = 4;
constexpr int PARAM_OFF = XB;
constexpr int SRD3 = 0x00020000;

#define WAIT_V(n) asm volatile("s_waitcnt vmcnt(%0)" ::"n"(n) : "memory")
#define WAIT_L0() asm volatile("s_waitcnt lgkmcnt(0)" ::: "memory")
#define BARRIER() do { asm volatile("" ::: "memory"); __builtin_amdgcn_s_barrier(); asm volatile("" ::: "memory"); } while (0)

#define ST(i)
#define STAMP_ARG
#define STAMP_PASS

__device__ __forceinline__ float bf2f(unsigned short v) { return __uint_as_float(((unsigned)v) << 16); }
__device__ __forceinline__ unsigned pack_bf16(float lo, float hi) {
  unsigned r;
  asm("v_cvt_pk_bf16_f32 %0, %1, %2" : "=v"(r) : "v"(lo), "v"(hi));
  return r;
}

struct Frags { s16x8 y0, y1, y2; s16x4 u0l, u0h, u1l, u1h, u2l, u2h; };

#define RD128(dst, addr, off) asm volatile("ds_read_b128 %0, %1 offset:%2" : "=v"(dst) : "v"(addr), "n"(off))
#define RDTR(dst, addr, off) asm volatile("ds_read_b64_tr_b16 %0, %1 offset:%2" : "=v"(dst) : "v"(addr), "n"(off))
#define CAT(l, h) __builtin_bit_cast(bf16x8, __builtin_shufflevector(l, h, 0, 1, 2, 3, 4, 5, 6, 7))
#define BC(v) __builtin_bit_cast(bf16x8, v)
#define SB() __builtin_amdgcn_sched_barrier(0)

template <int SP>
struct Cfg {
  static constexpr int S0 = SP ? 3 : 0, S1 = SP ? 2 : 1, S2 = SP ? 1 : 2;
  static constexpr int K0 = SP ? 2 : 0, K1 = 1, K2 = SP ? 0 : 2;
  static constexpr int SA = SP ? 2 : 1, SBo = SP ? 3 : 0;
};

#define WOFF(K, T) (XB + (K) * 8192 + (T) * 2048)
#define WM(n, a, b, c) asm volatile("s_waitcnt lgkmcnt(%3)\n\tv_mfma_f32_32x32x16_bf16 %0, %1, %2, %0" : "+v"(c) : "v"(a), "v"(b), "n"(n))
#define KSTEP(C, N, T)                                                                        \
  do {                                                                                        \
    WM(6, CAT(C.u0l, C.u0h), BC(C.y0), accA); SB();                                           \
    if ((T) == 0) { BARRIER(); SB(); }                                                        \
    RDTR(N.u0l, aw, WOFF(G::K0, T)); RDTR(N.u0h, aw, WOFF(G::K0, T) + 512);                   \
    RD128(N.y0, ax[T], G::S0 * 4096); SB();                                                   \
    WM(7, CAT(C.u1l, C.u1h), BC(C.y0), accB); SB();                                           \
    RDTR(N.u1l, aw, WOFF(G::K1, T)); RDTR(N.u1h, aw, WOFF(G::K1, T) + 512); SB();             \
    WM(8, CAT(C.u1l, C.u1h), BC(C.y1), accA); SB();                                           \
    RD128(N.y1, ax[T], G::S1 * 4096);                                                         \
    RDTR(N.u2l, aw, WOFF(G::K2, T)); RDTR(N.u2h, aw, WOFF(G::K2, T) + 512); SB();             \
    WM(9, CAT(C.u2l, C.u2h), BC(C.y1), accB); SB();                                           \
    RD128(N.y2, ax[T], G::S2 * 4096); SB();                                                   \
    WM(9, CAT(C.u2l, C.u2h), BC(C.y2), accA); SB();                                           \
  } while (0)

__device__ __forceinline__ void epi_half(const f32x16& acc, const f32x4 (&sc)[4], const f32x4 (&sh)[4], unsigned (&pk)[8]) {
#pragma unroll
  for (int i = 0; i < 8; ++i) {
    const f32x2 a = {acc[2 * i], acc[2 * i + 1]};
    const f32x2 s = {sc[i >> 1][(i & 1) * 2], sc[i >> 1][(i & 1) * 2 + 1]};
    const f32x2 f = {sh[i >> 1][(i & 1) * 2], sh[i >> 1][(i & 1) * 2 + 1]};
    const f32x2 y = a * s + f;
    const f32x2 t = y * -1.44269504088896340736f;
    f32x2 e = {__builtin_amdgcn_exp2f(t[0]), __builtin_amdgcn_exp2f(t[1])};
    e = e + 1.0f;
    const f32x2 r = {__builtin_amdgcn_rcpf(e[0]), __builtin_amdgcn_rcpf(e[1])};
    const f32x2 o = y * r;
    pk[i] = pack_bf16(o[0], o[1]);
  }
}

template <int SP>
__device__ __forceinline__ void consume(char* shm, int wid, int lane STAMP_ARG) {
  using G = Cfg<SP>;
  const int nh = wid & 1;
  const int h = lane >> 5, bl = lane & 31;
  const unsigned lbase = (unsigned)(size_t)(lds_char*)shm;
  unsigned ax[4];
#pragma unroll
  for (int t = 0; t < 4; ++t) ax[t] = lbase + bl * 128 + (((2 * t + h) ^ ((bl >> 1) & 7)) * 16);
  const int blk = (lane >> 4) & 1, q = (lane & 15) >> 2, p = lane & 3;
  unsigned aw = lbase + (8 * h + q) * 128 + (((4 * nh + 2 * (p & 1) + blk) ^ (((q >> 1) & 1) << 2)) * 16) + 8 * (p >> 1);
  const unsigned apar = lbase + PARAM_OFF + (32 * nh + 16 * h) * 4;
  const unsigned astg = lbase + bl * 128;
  const unsigned c0 = ((4 * nh + 2 * h) ^ (bl & 7)) * 16, c1 = ((4 * nh + 2 * h + 1) ^ (bl & 7)) * 16;

  f32x16 accA = {}, accB = {};
  Frags F0, F1;
  ST(1);
  BARRIER();
  ST(2);
  RDTR(F0.u0l, aw, WOFF(G::K0, 0)); RDTR(F0.u0h, aw, WOFF(G::K0, 0) + 512); RD128(F0.y0, ax[0], G::S0 * 4096);
  RDTR(F0.u1l, aw, WOFF(G::K1, 0)); RDTR(F0.u1h, aw, WOFF(G::K1, 0) + 512);
  RD128(F0.y1, ax[0], G::S1 * 4096); RDTR(F0.u2l, aw, WOFF(G::K2, 0)); RDTR(F0.u2h, aw, WOFF(G::K2, 0) + 512);
  RD128(F0.y2, ax[0], G::S2 * 4096);

#pragma unroll 1
  for (int st = 0; st < 7; ++st) {
    KSTEP(F0, F1, 1);
    KSTEP(F1, F0, 2);
    KSTEP(F0, F1, 3);
    const int delta = (st == 3) ? -3 * STAGE_B : STAGE_B;
#pragma unroll
    for (int t = 0; t < 4; ++t) ax[t] += delta;
    aw += delta;
    SB();
    KSTEP(F1, F0, 0);
  }
  KSTEP(F0, F1, 1);
  KSTEP(F1, F0, 2);
  KSTEP(F0, F1, 3);

  f32x4 sc[4], sh[4];
  WM(6, CAT(F1.u0l, F1.u0h), BC(F1.y0), accA); SB();
  RD128(sc[0], apar, 0); RD128(sh[0], apar, 256); SB();
  WM(6, CAT(F1.u1l, F1.u1h), BC(F1.y0), accB); SB();
  RD128(sc[1], apar, 16); RD128(sh[1], apar, 272); SB();
  WM(7, CAT(F1.u1l, F1.u1h), BC(F1.y1), accA); SB();
  RD128(sc[2], apar, 32); RD128(sh[2], apar, 288); SB();
  WM(7, CAT(F1.u2l, F1.u2h), BC(F1.y1), accB); SB();
  RD128(sc[3], apar, 48); RD128(sh[3], apar, 304); SB();
  WM(8, CAT(F1.u2l, F1.u2h), BC(F1.y2), accA); SB();
  asm volatile("s_waitcnt lgkmcnt(0)\n\ts_nop 7"
               : "+v"(sc[0]), "+v"(sc[1]), "+v"(sc[2]), "+v"(sc[3]), "+v"(sh[0]), "+v"(sh[1]), "+v"(sh[2]), "+v"(sh[3]), "+v"(accB));
  SB();
  ST(3);

  unsigned pk[8];
  epi_half(accB, sc, sh, pk);
  {
    const u32x4 v0 = {pk[0], pk[1], pk[2], pk[3]}, v1 = {pk[4], pk[5], pk[6], pk[7]};
    asm volatile("ds_write_b128 %0, %1 offset:%2" ::"v"(astg + c0), "v"(v0), "n"(G::SBo * 4096) : "memory");
    asm volatile("ds_write_b128 %0, %1 offset:%2" ::"v"(astg + c1), "v"(v1), "n"(G::SBo * 4096) : "memory");
  }
  SB();
  asm volatile("s_nop 3" : "+v"(accA));
  WAIT_L0();
  BARRIER();
  SB();
  ST(4);
  epi_half(accA, sc, sh, pk);
  {
    const u32x4 v0 = {pk[0], pk[1], pk[2], pk[3]}, v1 = {pk[4], pk[5], pk[6], pk[7]};
    asm volatile("ds_write_b128 %0, %1 offset:%2" ::"v"(astg + c0), "v"(v0), "n"(G::SA * 4096) : "memory");
    asm volatile("ds_write_b128 %0, %1 offset:%2" ::"v"(astg + c1), "v"(v1), "n"(G::SA * 4096) : "memory");
  }
  WAIT_L0();
  BARRIER();
  ST(5);
}

__global__ __launch_bounds__(PF ? 576 : 512) void conv_bn_silu(
    const char* __restrict__ x, const char* __restrict__ w, const unsigned short* __restrict__ mean,
    const unsigned short* __restrict__ var, const unsigned short* __restrict__ g,
    const unsigned short* __restrict__ bb, char* __restrict__ out STAMP_ARG) {
  __shared__ __attribute__((aligned(1024))) char shm[NSLOT * STAGE_B];
  const int tid = threadIdx.x;
  const int wid = __builtin_amdgcn_readfirstlane(tid >> 6);
  const int lane = tid & 63;
  ST(0);

  if (wid < 4) {
    if (wid & 2) consume<1>(shm, wid, lane STAMP_PASS);
    else consume<0>(shm, wid, lane STAMP_PASS);
    return;
  }

  const int xcd = blockIdx.x & 7, idx = blockIdx.x >> 3;
  const int mt = (xcd >> 1) * 8 + (idx & 7);
  const int nt = (xcd & 1) * 4 + (idx >> 3);
  const int b0 = mt * 32, n0 = nt * 64;

  const int rot = ROT == 8 ? ((idx & 7) + 2 * (idx >> 3)) : ROT == 4 ? 2 * ((idx + (idx >> 3)) & 3) : ROT == 2 ? 4 * ((idx + (idx >> 3)) & 1) : 0;
  const int lw = wid - 4;
  const __amdgpu_buffer_rsrc_t rx = __builtin_amdgcn_make_buffer_rsrc((void*)x, 0, 1024 * 4 * 512 * 2, SRD3);
  const __amdgpu_buffer_rsrc_t rw = __builtin_amdgcn_make_buffer_rsrc((void*)w, 0, 3 * 512 * 512 * 2, SRD3);
  const int lr = lane >> 3, ls = lane & 7;
  const int vx0 = lr * 4096 + ((ls ^ (lane >> 4)) * 16);
  const int vx1 = lr * 4096 + ((ls ^ (4 + (lane >> 4))) * 16);
  const int vw = lr * 1024 + ((ls ^ (((lane >> 4) & 1) << 2)) * 16);
  const int sx = (b0 * 4 + lw) * 1024;
  auto stage = [&](int st, int slotoff) {
    const int cs = (st + rot) & 7;
    lds_char* slot = (lds_char*)shm + slotoff;
#pragma unroll
    for (int j = 0; j < 4; ++j)
      __builtin_amdgcn_raw_ptr_buffer_load_lds(rx, (lds_void*)(slot + (lw * 4 + j) * 1024), 16, (j & 1) ? vx1 : vx0,
                                               sx + j * 8 * 4096 + cs * 128, 0, LDAUX);
#pragma unroll
    for (int j = 0; j < 6; ++j) {
      const int r = lw * 48 + j * 8;
      const int k = r >> 6, cc = r & 63;
      __builtin_amdgcn_raw_ptr_buffer_load_lds(rw, (lds_void*)(slot + XB + (lw * 6 + j) * 1024), 16, vw,
                                               (k * 512 + cs * 64 + cc) * 1024 + n0 * 2, 0, LDAUX);
    }
  };
  const unsigned short pm = mean[n0 + lane], pv = var[n0 + lane], pg = g[n0 + lane], pb = bb[n0 + lane];
  SB();
  stage(0, 0);
  ST(1);
  stage(1, STAGE_B);
  ST(2);
  WAIT_V(10);
  ST(3);
  BARRIER();
  stage(2, 2 * STAGE_B);
  const float scale = bf2f(pg) * __builtin_amdgcn_rsqf(bf2f(pv) + 1e-5f);
  const float shift = bf2f(pb) - bf2f(pm) * scale;
  int slotoff = 3 * STAGE_B;
#pragma unroll 1
  for (int k = 1; k <= 5; ++k) {
    WAIT_V(10);
    BARRIER();
    stage(k + 2, slotoff);
    slotoff = (slotoff == 3 * STAGE_B) ? 0 : slotoff + STAGE_B;
  }
  ST(4);
  WAIT_V(10);
  BARRIER();
  if (lw == 0) {
    const unsigned a = (unsigned)(size_t)(lds_char*)shm + PARAM_OFF + lane * 4;
    asm volatile("ds_write_b32 %0, %1\n\tds_write_b32 %0, %2 offset:256" ::"v"(a), "v"(scale), "v"(shift) : "memory");
  }
  WAIT_V(0);
  WAIT_L0();
  ST(5);
  BARRIER();

  const __amdgpu_buffer_rsrc_t ro = __builtin_amdgcn_make_buffer_rsrc((void*)out, 0, 1024 * 4 * 512 * 2, SRD3);
  const unsigned sbase = (unsigned)(size_t)(lds_char*)shm + (lw * 8 + lr) * 128 + ((ls ^ lr) * 16);
  const int vo = (b0 + lw * 8 + lr) * 4096 + n0 * 2 + ls * 16;
#pragma unroll
  for (int ph = 0; ph < 2; ++ph) {
    BARRIER();
    const int sa = ph ? 1 : 0, sb = ph ? 2 : 3;
    u32x4 va, vb;
    asm volatile("ds_read_b128 %0, %2 offset:%3\n\tds_read_b128 %1, %2 offset:%4\n\ts_waitcnt lgkmcnt(0)"
                 : "=&v"(va), "=&v"(vb) : "v"(sbase), "n"(sa * 4096), "n"(sb * 4096) : "memory");
    __builtin_amdgcn_raw_buffer_store_b128(va, ro, vo, sa * 1024, STAUX);
    __builtin_amdgcn_raw_buffer_store_b128(vb, ro, vo, sb * 1024, STAUX);
    ST(6 + ph);
  }
}

extern "C" void kernel_launch(void* const* d_in, const int* in_sizes, int n_in, void* d_out, int out_size,
                              void* d_ws, size_t ws_size, hipStream_t stream) {
  conv_bn_silu<<<256, PF ? 576 : 512, 0, stream>>>((const char*)d_in[0], (const char*)d_in[1], (const unsigned short*)d_in[2],
                                        (const unsigned short*)d_in[3], (const unsigned short*)d_in[4],
                                        (const unsigned short*)d_in[5], (char*)d_out
                                        );
}
```

```cpp
#define PROBE 0
#include <hip/hip_runtime.h>
#include <hip/hip_bf16.h>
#include <stdint.h>

typedef __attribute__((ext_vector_type(8))) __bf16 bf16x8;
typedef __attribute__((ext_vector_type(4))) short s16x4;
typedef __attribute__((ext_vector_type(8))) short s16x8;
typedef __attribute__((ext_vector_type(16))) float f32x16;
typedef __attribute__((ext_vector_type(4))) float f32x4;
typedef __attribute__((ext_vector_type(2))) float f32x2;
typedef __attribute__((ext_vector_type(4))) unsigned u32x4;
typedef __attribute__((address_space(3))) char lds_char;
typedef __attribute__((address_space(3))) void lds_void;

#define ROT 4
#define PF 0
#define PFS 0
#define PFD 3
#define LDAUX 0
#define STAUX 2

constexpr int STAGE_B = 40960;
constexpr int XB = 16384;
constexpr int NSLOT = 4;
constexpr int PARAM_OFF = XB;
constexpr int SRD3 = 0x00020000;

#define WAIT_V(n) asm volatile("s_waitcnt vmcnt(%0)" ::"n"(n) : "memory")
#define WAIT_L0() asm volatile("s_waitcnt lgkmcnt(0)" ::: "memory")
#define BARRIER() do { asm volatile("" ::: "memory"); __builtin_amdgcn_s_barrier(); asm volatile("" ::: "memory"); } while (0)

#define ST(i)
#define STAMP_ARG
#define STAMP_PASS

__device__ __forceinline__ float bf2f(unsigned short v) { return __uint_as_float(((unsigned)v) << 16); }
__device__ __forceinline__ unsigned pack_bf16(float lo, float hi) {
  unsigned r;
  asm("v_cvt_pk_bf16_f32 %0, %1, %2" : "=v"(r) : "v"(lo), "v"(hi));
  return r;
}

struct Frags { s16x8 y0, y1, y2; s16x4 u0l, u0h, u1l, u1h, u2l, u2h; };

#define RD128(dst, addr, off) asm volatile("ds_read_b128 %0, %1 offset:%2" : "=v"(dst) : "v"(addr), "n"(off))
#define RDTR(dst, addr, off) asm volatile("ds_read_b64_tr_b16 %0, %1 offset:%2" : "=v"(dst) : "v"(addr), "n"(off))
#define CAT(l, h) __builtin_bit_cast(bf16x8, __builtin_shufflevector(l, h, 0, 1, 2, 3, 4, 5, 6, 7))
#define BC(v) __builtin_bit_cast(bf16x8, v)
#define SB() __builtin_amdgcn_sched_barrier(0)

template <int SP>
struct Cfg {
  static constexpr int S0 = SP ? 3 : 0, S1 = SP ? 2 : 1, S2 = SP ? 1 : 2;
  static constexpr int K0 = SP ? 2 : 0, K1 = 1, K2 = SP ? 0 : 2;
  static constexpr int SA = SP ? 2 : 1, SBo = SP ? 3 : 0;
};

#define WOFF(K, T) (XB + (K) * 8192 + (T) * 2048)
#define WM(n, a, b, c) asm volatile("s_waitcnt lgkmcnt(%3)\n\tv_mfma_f32_32x32x16_bf16 %0, %1, %2, %0" : "+v"(c) : "v"(a), "v"(b), "n"(n))
#define KSTEP(C, N, T)                                                                        \
  do {                                                                                        \
    WM(6, CAT(C.u0l, C.u0h), BC(C.y0), accA); SB();                                           \
    if ((T) == 0) { BARRIER(); SB(); }                                                        \
    RDTR(N.u0l, aw, WOFF(G::K0, T)); RDTR(N.u0h, aw, WOFF(G::K0, T) + 512);                   \
    RD128(N.y0, ax[T], G::S0 * 4096); SB();                                                   \
    WM(7, CAT(C.u1l, C.u1h), BC(C.y0), accB); SB();                                           \
    RDTR(N.u1l, aw, WOFF(G::K1, T)); RDTR(N.u1h, aw, WOFF(G::K1, T) + 512); SB();             \
    WM(8, CAT(C.u1l, C.u1h), BC(C.y1), accA); SB();                                           \
    RD128(N.y1, ax[T], G::S1 * 4096);                                                         \
    RDTR(N.u2l, aw, WOFF(G::K2, T)); RDTR(N.u2h, aw, WOFF(G::K2, T) + 512); SB();             \
    WM(9, CAT(C.u2l, C.u2h), BC(C.y1), accB); SB();                                           \
    RD128(N.y2, ax[T], G::S2 * 4096); SB();                                                   \
    WM(9, CAT(C.u2l, C.u2h), BC(C.y2), accA); SB();                                           \
  } while (0)

__device__ __forceinline__ void epi_half(const f32x16& acc, const f32x4 (&sc)[4], const f32x4 (&sh)[4], unsigned (&pk)[8]) {
#pragma unroll
  for (int i = 0; i < 8; ++i) {
    const f32x2 a = {acc[2 * i], acc[2 * i + 1]};
    const f32x2 s = {sc[i >> 1][(i & 1) * 2], sc[i >> 1][(i & 1) * 2 + 1]};
    const f32x2 f = {sh[i >> 1][(i & 1) * 2], sh[i >> 1][(i & 1) * 2 + 1]};
    const f32x2 y = a * s + f;
    const f32x2 t = y * -1.44269504088896340736f;
    f32x2 e = {__builtin_amdgcn_exp2f(t[0]), __builtin_amdgcn_exp2f(t[1])};
    e = e + 1.0f;
    const f32x2 r = {__builtin_amdgcn_rcpf(e[0]), __builtin_amdgcn_rcpf(e[1])};
    const f32x2 o = y * r;
    pk[i] = pack_bf16(o[0], o[1]);
  }
}

template <int SP>
__device__ __forceinline__ void consume(char* shm, int wid, int lane STAMP_ARG) {
  using G = Cfg<SP>;
  const int nh = wid & 1;
  const int h = lane >> 5, bl = lane & 31;
  const unsigned lbase = (unsigned)(size_t)(lds_char*)shm;
  unsigned ax[4];
#pragma unroll
  for (int t = 0; t < 4; ++t) ax[t] = lbase + bl * 128 + (((2 * t + h) ^ ((bl >> 1) & 7)) * 16);
  const int blk = (lane >> 4) & 1, q = (lane & 15) >> 2, p = lane & 3;
  unsigned aw = lbase + (8 * h + q) * 128 + (((4 * nh + 2 * (p & 1) + blk) ^ (((q >> 1) & 1) << 2)) * 16) + 8 * (p >> 1);
  const unsigned apar = lbase + PARAM_OFF + (32 * nh + 16 * h) * 4;
  const unsigned astg = lbase + bl * 128;
  const unsigned c0 = ((4 * nh + 2 * h) ^ (bl & 7)) * 16, c1 = ((4 * nh + 2 * h + 1) ^ (bl & 7)) * 16;

  f32x16 accA = {}, accB = {};
  Frags F0, F1;
  ST(1);
  BARRIER();
  ST(2);
  RDTR(F0.u0l, aw, WOFF(G::K0, 0)); RDTR(F0.u0h, aw, WOFF(G::K0, 0) + 512); RD128(F0.y0, ax[0], G::S0 * 4096);
  RDTR(F0.u1l, aw, WOFF(G::K1, 0)); RDTR(F0.u1h, aw, WOFF(G::K1, 0) + 512);
  RD128(F0.y1, ax[0], G::S1 * 4096); RDTR(F0.u2l, aw, WOFF(G::K2, 0)); RDTR(F0.u2h, aw, WOFF(G::K2, 0) + 512);
  RD128(F0.y2, ax[0], G::S2 * 4096);

#pragma unroll 1
  for (int st = 0; st < 7; ++st) {
    KSTEP(F0, F1, 1);
    KSTEP(F1, F0, 2);
    KSTEP(F0, F1, 3);
    const int delta = (st == 3) ? -3 * STAGE_B : STAGE_B;
#pragma unroll
    for (int t = 0; t < 4; ++t) ax[t] += delta;
    aw += delta;
    SB();
    KSTEP(F1, F0, 0);
  }
  KSTEP(F0, F1, 1);
  KSTEP(F1, F0, 2);
  KSTEP(F0, F1, 3);

  f32x4 sc[4], sh[4];
  WM(6, CAT(F1.u0l, F1.u0h), BC(F1.y0), accA); SB();
  RD128(sc[0], apar, 0); RD128(sh[0], apar, 256); SB();
  WM(6, CAT(F1.u1l, F1.u1h), BC(F1.y0), accB); SB();
  RD128(sc[1], apar, 16); RD128(sh[1], apar, 272); SB();
  WM(7, CAT(F1.u1l, F1.u1h), BC(F1.y1), accA); SB();
  RD128(sc[2], apar, 32); RD128(sh[2], apar, 288); SB();
  WM(7, CAT(F1.u2l, F1.u2h), BC(F1.y1), accB); SB();
  RD128(sc[3], apar, 48); RD128(sh[3], apar, 304); SB();
  WM(8, CAT(F1.u2l, F1.u2h), BC(F1.y2), accA); SB();
  asm volatile("s_waitcnt lgkmcnt(0)\n\ts_nop 7"
               : "+v"(sc[0]), "+v"(sc[1]), "+v"(sc[2]), "+v"(sc[3]), "+v"(sh[0]), "+v"(sh[1]), "+v"(sh[2]), "+v"(sh[3]), "+v"(accB));
  SB();
  ST(3);

  unsigned pk[8];
  epi_half(accB, sc, sh, pk);
  {
    const u32x4 v0 = {pk[0], pk[1], pk[2], pk[3]}, v1 = {pk[4], pk[5], pk[6], pk[7]};
    asm volatile("ds_write_b128 %0, %1 offset:%2" ::"v"(astg + c0), "v"(v0), "n"(G::SBo * 4096) : "memory");
    asm volatile("ds_write_b128 %0, %1 offset:%2" ::"v"(astg + c1), "v"(v1), "n"(G::SBo * 4096) : "memory");
  }
  SB();
  asm volatile("s_nop 3" : "+v"(accA));
  WAIT_L0();
  BARRIER();
  SB();
  ST(4);
  epi_half(accA, sc, sh, pk);
  {
    const u32x4 v0 = {pk[0], pk[1], pk[2], pk[3]}, v1 = {pk[4], pk[5], pk[6], pk[7]};
    asm volatile("ds_write_b128 %0, %1 offset:%2" ::"v"(astg + c0), "v"(v0), "n"(G::SA * 4096) : "memory");
    asm volatile("ds_write_b128 %0, %1 offset:%2" ::"v"(astg + c1), "v"(v1), "n"(G::SA * 4096) : "memory");
  }
  WAIT_L0();
  BARRIER();
  ST(5);
}

__global__ __launch_bounds__(PF ? 576 : 512) void conv_bn_silu(
    const char* __restrict__ x, const char* __restrict__ w, const unsigned short* __restrict__ mean,
    const unsigned short* __restrict__ var, const unsigned short* __restrict__ g,
    const unsigned short* __restrict__ bb, char* __restrict__ out STAMP_ARG) {
  __shared__ __attribute__((aligned(1024))) char shm[NSLOT * STAGE_B];
  const int tid = threadIdx.x;
  const int wid = __builtin_amdgcn_readfirstlane(tid >> 6);
  const int lane = tid & 63;
  ST(0);

  if (wid < 4) {
    if (wid & 2) consume<1>(shm, wid, lane STAMP_PASS);
    else consume<0>(shm, wid, lane STAMP_PASS);
    return;
  }

  const int xcd = blockIdx.x & 7, idx = blockIdx.x >> 3;
  const int mt = (xcd >> 1) * 8 + (idx & 7);
  const int nt = (xcd & 1) * 4 + (idx >> 3);
  const int b0 = mt * 32, n0 = nt * 64;

  const unsigned long long T0 = __builtin_amdgcn_s_memrealtime();
  const int rot = ROT == 8 ? ((idx & 7) + 2 * (idx >> 3)) : ROT == 4 ? 2 * ((idx + (idx >> 3)) & 3) : ROT == 2 ? 4 * ((idx + (idx >> 3)) & 1) : 0;
  const int lw = wid - 4;
  const __amdgpu_buffer_rsrc_t rx = __builtin_amdgcn_make_buffer_rsrc((void*)x, 0, 1024 * 4 * 512 * 2, SRD3);
  const __amdgpu_buffer_rsrc_t rw = __builtin_amdgcn_make_buffer_rsrc((void*)w, 0, 3 * 512 * 512 * 2, SRD3);
  const int lr = lane >> 3, ls = lane & 7;
  const int vx0 = lr * 4096 + ((ls ^ (lane >> 4)) * 16);
  const int vx1 = lr * 4096 + ((ls ^ (4 + (lane >> 4))) * 16);
  const int vw = lr * 1024 + ((ls ^ (((lane >> 4) & 1) << 2)) * 16);
  const int sx = (b0 * 4 + lw) * 1024;
  auto stage = [&](int st, int slotoff) {
    const int cs = (st + rot) & 7;
    lds_char* slot = (lds_char*)shm + slotoff;
#pragma unroll
    for (int j = 0; j < 4; ++j)
      __builtin_amdgcn_raw_ptr_buffer_load_lds(rx, (lds_void*)(slot + (lw * 4 + j) * 1024), 16, (j & 1) ? vx1 : vx0,
                                               sx + j * 8 * 4096 + cs * 128, 0, LDAUX);
#pragma unroll
    for (int j = 0; j < 6; ++j) {
      const int r = lw * 48 + j * 8;
      const int k = r >> 6, cc = r & 63;
      __builtin_amdgcn_raw_ptr_buffer_load_lds(rw, (lds_void*)(slot + XB + (lw * 6 + j) * 1024), 16, vw,
                                               (k * 512 + cs * 64 + cc) * 1024 + n0 * 2, 0, LDAUX);
    }
  };
  const unsigned short pm = mean[n0 + lane], pv = var[n0 + lane], pg = g[n0 + lane], pb = bb[n0 + lane];
  SB();
  stage(0, 0);
  ST(1);
  stage(1, STAGE_B);
  ST(2);
  WAIT_V(10);
  ST(3);
  BARRIER();
  const unsigned long long T1 = __builtin_amdgcn_s_memrealtime();
  stage(2, 2 * STAGE_B);
  const float scale = bf2f(pg) * __builtin_amdgcn_rsqf(bf2f(pv) + 1e-5f);
  const float shift = bf2f(pb) - bf2f(pm) * scale;
  int slotoff = 3 * STAGE_B;
#pragma unroll 1
  for (int k = 1; k <= 5; ++k) {
    WAIT_V(10);
    BARRIER();
    stage(k + 2, slotoff);
    slotoff = (slotoff == 3 * STAGE_B) ? 0 : slotoff + STAGE_B;
  }
  ST(4);
  WAIT_V(10);
  BARRIER();
  if (lw == 0) {
    const unsigned a = (unsigned)(size_t)(lds_char*)shm + PARAM_OFF + lane * 4;
    asm volatile("ds_write_b32 %0, %1\n\tds_write_b32 %0, %2 offset:256" ::"v"(a), "v"(scale), "v"(shift) : "memory");
  }
  WAIT_V(0);
  WAIT_L0();
  ST(5);
  BARRIER();

  const __amdgpu_buffer_rsrc_t ro = __builtin_amdgcn_make_buffer_rsrc((void*)out, 0, 1024 * 4 * 512 * 2, SRD3);
  const unsigned sbase = (unsigned)(size_t)(lds_char*)shm + (lw * 8 + lr) * 128 + ((ls ^ lr) * 16);
  const int vo = (b0 + lw * 8 + lr) * 4096 + n0 * 2 + ls * 16;
  unsigned long long T2 = 0;
#pragma unroll
  for (int ph = 0; ph < 2; ++ph) {
    BARRIER();
    if (ph == 0) T2 = __builtin_amdgcn_s_memrealtime();
    const int sa = ph ? 1 : 0, sb = ph ? 2 : 3;
    u32x4 va, vb;
    asm volatile("ds_read_b128 %0, %2 offset:%3\n\tds_read_b128 %1, %2 offset:%4\n\ts_waitcnt lgkmcnt(0)"
                 : "=&v"(va), "=&v"(vb) : "v"(sbase), "n"(sa * 4096), "n"(sb * 4096) : "memory");
    __builtin_amdgcn_raw_buffer_store_b128(va, ro, vo, sa * 1024, STAUX);
    __builtin_amdgcn_raw_buffer_store_b128(vb, ro, vo, sb * 1024, STAUX);
    ST(6 + ph);
  }
  {
    const unsigned long long T3 = __builtin_amdgcn_s_memrealtime();
    const unsigned long long d = PROBE == 1 ? 4ull * (T1 - T0) : PROBE == 2 ? 4ull * (T2 - T1) : PROBE == 3 ? 4ull * (T3 - T2) : PROBE == 4 ? 4ull * (T3 - T0) : 0ull * (T1 - T0 + T2);
    while (__builtin_amdgcn_s_memrealtime() - T3 < d) {}
  }
}

extern "C" void kernel_launch(void* const* d_in, const int* in_sizes, int n_in, void* d_out, int out_size,
                              void* d_ws, size_t ws_size, hipStream_t stream) {
  conv_bn_silu<<<256, PF ? 576 : 512, 0, stream>>>((const char*)d_in[0], (const char*)d_in[1], (const unsigned short*)d_in[2],
                                        (const unsigned short*)d_in[3], (const unsigned short*)d_in[4],
                                        (const unsigned short*)d_in[5], (char*)d_out
                                        );
}
```

```cpp
#define PROBE 0
#include <hip/hip_runtime.h>
#include <hip/hip_bf16.h>
#include <stdint.h>

typedef __attribute__((ext_vector_type(8))) __bf16 bf16x8;
typedef __attribute__((ext_vector_type(4))) short s16x4;
typedef __attribute__((ext_vector_type(8))) short s16x8;
typedef __attribute__((ext_vector_type(16))) float f32x16;
typedef __attribute__((ext_vector_type(4))) float f32x4;
typedef __attribute__((ext_vector_type(2))) float f32x2;
typedef __attribute__((ext_vector_type(4))) unsigned u32x4;
typedef __attribute__((address_space(3))) char lds_char;
typedef __attribute__((address_space(3))) void lds_void;

#define ROT 4
#define PF 0
#define PFS 0
#define PFD 3
#define LDAUX 0
#define STAUX 2

constexpr int STAGE_B = 40960;
constexpr int XB = 16384;
constexpr int NSLOT = 4;
constexpr int PARAM_OFF = XB;
constexpr int SRD3 = 0x00020000;

#define WAIT_V(n) asm volatile("s_waitcnt vmcnt(%0)" ::"n"(n) : "memory")
#define WAIT_L0() asm volatile("s_waitcnt lgkmcnt(0)" ::: "memory")
#define BARRIER() do { asm volatile("" ::: "memory"); __builtin_amdgcn_s_barrier(); asm volatile("" ::: "memory"); } while (0)

#define ST(i)
#define STAMP_ARG
#define STAMP_PASS

__device__ __forceinline__ float bf2f(unsigned short v) { return __uint_as_float(((unsigned)v) << 16); }
__device__ __forceinline__ unsigned pack_bf16(float lo, float hi) {
  unsigned r;
  asm("v_cvt_pk_bf16_f32 %0, %1, %2" : "=v"(r) : "v"(lo), "v"(hi));
  return r;
}

struct Frags { s16x8 y0, y1, y2; s16x4 u0l, u0h, u1l, u1h, u2l, u2h; };

#define RD128(dst, addr, off) asm volatile("ds_read_b128 %0, %1 offset:%2" : "=v"(dst) : "v"(addr), "n"(off))
#define RDTR(dst, addr, off) asm volatile("ds_read_b64_tr_b16 %0, %1 offset:%2" : "=v"(dst) : "v"(addr), "n"(off))
#define CAT(l, h) __builtin_bit_cast(bf16x8, __builtin_shufflevector(l, h, 0, 1, 2, 3, 4, 5, 6, 7))
#define BC(v) __builtin_bit_cast(bf16x8, v)
#define SB() __builtin_amdgcn_sched_barrier(0)

template <int SP>
struct Cfg {
  static constexpr int S0 = SP ? 3 : 0, S1 = SP ? 2 : 1, S2 = SP ? 1 : 2;
  static constexpr int K0 = SP ? 2 : 0, K1 = 1, K2 = SP ? 0 : 2;
  static constexpr int SA = SP ? 2 : 1, SBo = SP ? 3 : 0;
};

#define WOFF(K, T) (XB + (K) * 8192 + (T) * 2048)
#define WM(n, a, b, c) asm volatile("s_waitcnt lgkmcnt(%3)\n\tv_mfma_f32_32x32x16_bf16 %0, %1, %2, %0" : "+v"(c) : "v"(a), "v"(b), "n"(n))
#define KSTEP(C, N, T)                                                                        \
  do {                                                                                        \
    WM(6, CAT(C.u0l, C.u0h), BC(C.y0), accA); SB();                                           \
    if ((T) == 0) { BARRIER(); SB(); }                                                        \
    RDTR(N.u0l, aw, WOFF(G::K0, T)); RDTR(N.u0h, aw, WOFF(G::K0, T) + 512);                   \
    RD128(N.y0, ax[T], G::S0 * 4096); SB();                                                   \
    WM(7, CAT(C.u1l, C.u1h), BC(C.y0), accB); SB();                                           \
    RDTR(N.u1l, aw, WOFF(G::K1, T)); RDTR(N.u1h, aw, WOFF(G::K1, T) + 512); SB();             \
    WM(8, CAT(C.u1l, C.u1h), BC(C.y1), accA); SB();                                           \
    RD128(N.y1, ax[T], G::S1 * 4096);                                                         \
    RDTR(N.u2l, aw, WOFF(G::K2, T)); RDTR(N.u2h, aw, WOFF(G::K2, T) + 512); SB();             \
    WM(9, CAT(C.u2l, C.u2h), BC(C.y1), accB); SB();                                           \
    RD128(N.y2, ax[T], G::S2 * 4096); SB();                                                   \
    WM(9, CAT(C.u2l, C.u2h), BC(C.y2), accA); SB();                                           \
  } while (0)

__device__ __forceinline__ void epi_half(const f32x16& acc, const f32x4 (&sc)[4], const f32x4 (&sh)[4], unsigned (&pk)[8]) {
#pragma unroll
  for (int i = 0; i < 8; ++i) {
    const f32x2 a = {acc[2 * i], acc[2 * i + 1]};
    const f32x2 s = {sc[i >> 1][(i & 1) * 2], sc[i >> 1][(i & 1) * 2 + 1]};
    const f32x2 f = {sh[i >> 1][(i & 1) * 2], sh[i >> 1][(i & 1) * 2 + 1]};
    const f32x2 y = a * s + f;
    const f32x2 t = y * -1.44269504088896340736f;
    f32x2 e = {__builtin_amdgcn_exp2f(t[0]), __builtin_amdgcn_exp2f(t[1])};
    e = e + 1.0f;
    const f32x2 r = {__builtin_amdgcn_rcpf(e[0]), __builtin_amdgcn_rcpf(e[1])};
    const f32x2 o = y * r;
    pk[i] = pack_bf16(o[0], o[1]);
  }
}

template <int SP>
__device__ __forceinline__ void consume(char* shm, int wid, int lane STAMP_ARG) {
  using G = Cfg<SP>;
  const int nh = wid & 1;
  const int h = lane >> 5, bl = lane & 31;
  const unsigned lbase = (unsigned)(size_t)(lds_char*)shm;
  unsigned ax[4];
#pragma unroll
  for (int t = 0; t < 4; ++t) ax[t] = lbase + bl * 128 + (((2 * t + h) ^ ((bl >> 1) & 7)) * 16);
  const int blk = (lane >> 4) & 1, q = (lane & 15) >> 2, p = lane & 3;
  unsigned aw = lbase + (8 * h + q) * 128 + (((4 * nh + 2 * (p & 1) + blk) ^ (((q >> 1) & 1) << 2)) * 16) + 8 * (p >> 1);
  const unsigned apar = lbase + PARAM_OFF + (32 * nh + 16 * h) * 4;
  const unsigned astg = lbase + bl * 128;
  const unsigned c0 = ((4 * nh + 2 * h) ^ (bl & 7)) * 16, c1 = ((4 * nh + 2 * h + 1) ^ (bl & 7)) * 16;

  f32x16 accA = {}, accB = {};
  Frags F0, F1;
  ST(1);
  BARRIER();
  ST(2);
  RDTR(F0.u0l, aw, WOFF(G::K0, 0)); RDTR(F0.u0h, aw, WOFF(G::K0, 0) + 512); RD128(F0.y0, ax[0], G::S0 * 4096);
  RDTR(F0.u1l, aw, WOFF(G::K1, 0)); RDTR(F0.u1h, aw, WOFF(G::K1, 0) + 512);
  RD128(F0.y1, ax[0], G::S1 * 4096); RDTR(F0.u2l, aw, WOFF(G::K2, 0)); RDTR(F0.u2h, aw, WOFF(G::K2, 0) + 512);
  RD128(F0.y2, ax[0], G::S2 * 4096);

#pragma unroll 1
  for (int st = 0; st < 7; ++st) {
    KSTEP(F0, F1, 1);
    KSTEP(F1, F0, 2);
    KSTEP(F0, F1, 3);
    const int delta = (st == 3) ? -3 * STAGE_B : STAGE_B;
#pragma unroll
    for (int t = 0; t < 4; ++t) ax[t] += delta;
    aw += delta;
    SB();
    KSTEP(F1, F0, 0);
  }
  KSTEP(F0, F1, 1);
  KSTEP(F1, F0, 2);
  KSTEP(F0, F1, 3);

  f32x4 sc[4], sh[4];
  WM(6, CAT(F1.u0l, F1.u0h), BC(F1.y0), accA); SB();
  RD128(sc[0], apar, 0); RD128(sh[0], apar, 256); SB();
  WM(6, CAT(F1.u1l, F1.u1h), BC(F1.y0), accB); SB();
  RD128(sc[1], apar, 16); RD128(sh[1], apar, 272); SB();
  WM(7, CAT(F1.u1l, F1.u1h), BC(F1.y1), accA); SB();
  RD128(sc[2], apar, 32); RD128(sh[2], apar, 288); SB();
  WM(7, CAT(F1.u2l, F1.u2h), BC(F1.y1), accB); SB();
  RD128(sc[3], apar, 48); RD128(sh[3], apar, 304); SB();
  WM(8, CAT(F1.u2l, F1.u2h), BC(F1.y2), accA); SB();
  asm volatile("s_waitcnt lgkmcnt(0)\n\ts_nop 7"
               : "+v"(sc[0]), "+v"(sc[1]), "+v"(sc[2]), "+v"(sc[3]), "+v"(sh[0]), "+v"(sh[1]), "+v"(sh[2]), "+v"(sh[3]), "+v"(accB));
  SB();
  ST(3);

  unsigned pk[8];
  epi_half(accB, sc, sh, pk);
  {
    const u32x4 v0 = {pk[0], pk[1], pk[2], pk[3]}, v1 = {pk[4], pk[5], pk[6], pk[7]};
    asm volatile("ds_write_b128 %0, %1 offset:%2" ::"v"(astg + c0), "v"(v0), "n"(G::SBo * 4096) : "memory");
    asm volatile("ds_write_b128 %0, %1 offset:%2" ::"v"(astg + c1), "v"(v1), "n"(G::SBo * 4096) : "memory");
  }
  SB();
  asm volatile("s_nop 3" : "+v"(accA));
  WAIT_L0();
  BARRIER();
  SB();
  ST(4);
  epi_half(accA, sc, sh, pk);
  {
    const u32x4 v0 = {pk[0], pk[1], pk[2], pk[3]}, v1 = {pk[4], pk[5], pk[6], pk[7]};
    asm volatile("ds_write_b128 %0, %1 offset:%2" ::"v"(astg + c0), "v"(v0), "n"(G::SA * 4096) : "memory");
    asm volatile("ds_write_b128 %0, %1 offset:%2" ::"v"(astg + c1), "v"(v1), "n"(G::SA * 4096) : "memory");
  }
  WAIT_L0();
  BARRIER();
  ST(5);
}

__global__ __launch_bounds__(PF ? 576 : 512) void conv_bn_silu(
    const char* __restrict__ x, const char* __restrict__ w, const unsigned short* __restrict__ mean,
    const unsigned short* __restrict__ var, const unsigned short* __restrict__ g,
    const unsigned short* __restrict__ bb, char* __restrict__ out STAMP_ARG) {
  __shared__ __attribute__((aligned(1024))) char shm[NSLOT * STAGE_B];
  const int tid = threadIdx.x;
  const int wid = __builtin_amdgcn_readfirstlane(tid >> 6);
  const int lane = tid & 63;
  ST(0);
  unsigned long long T0a;
  asm volatile("s_memrealtime %0\n\ts_waitcnt lgkmcnt(0)" : "=s"(T0a) :: "memory");
  SB();

  if (wid < 4) {
    if (wid & 2) consume<1>(shm, wid, lane STAMP_PASS);
    else consume<0>(shm, wid, lane STAMP_PASS);
    return;
  }

  const int xcd = blockIdx.x & 7, idx = blockIdx.x >> 3;
  const int mt = (xcd >> 1) * 8 + (idx & 7);
  const int nt = (xcd & 1) * 4 + (idx >> 3);
  const int b0 = mt * 32, n0 = nt * 64;

  unsigned long long T0b;
  asm volatile("s_memrealtime %0\n\ts_waitcnt lgkmcnt(0)" : "=s"(T0b) : "s"(x), "s"(out), "s"(g) : "memory");
  SB();
  const int rot = ROT == 8 ? ((idx & 7) + 2 * (idx >> 3)) : ROT == 4 ? 2 * ((idx + (idx >> 3)) & 3) : ROT == 2 ? 4 * ((idx + (idx >> 3)) & 1) : 0;
  const int lw = wid - 4;
  const __amdgpu_buffer_rsrc_t rx = __builtin_amdgcn_make_buffer_rsrc((void*)x, 0, 1024 * 4 * 512 * 2, SRD3);
  const __amdgpu_buffer_rsrc_t rw = __builtin_amdgcn_make_buffer_rsrc((void*)w, 0, 3 * 512 * 512 * 2, SRD3);
  const int lr = lane >> 3, ls = lane & 7;
  const int vx0 = lr * 4096 + ((ls ^ (lane >> 4)) * 16);
  const int vx1 = lr * 4096 + ((ls ^ (4 + (lane >> 4))) * 16);
  const int vw = lr * 1024 + ((ls ^ (((lane >> 4) & 1) << 2)) * 16);
  const int sx = (b0 * 4 + lw) * 1024;
  auto stage = [&](int st, int slotoff) {
    const int cs = (st + rot) & 7;
    lds_char* slot = (lds_char*)shm + slotoff;
#pragma unroll
    for (int j = 0; j < 4; ++j)
      __builtin_amdgcn_raw_ptr_buffer_load_lds(rx, (lds_void*)(slot + (lw * 4 + j) * 1024), 16, (j & 1) ? vx1 : vx0,
                                               sx + j * 8 * 4096 + cs * 128, 0, LDAUX);
#pragma unroll
    for (int j = 0; j < 6; ++j) {
      const int r = lw * 48 + j * 8;
      const int k = r >> 6, cc = r & 63;
      __builtin_amdgcn_raw_ptr_buffer_load_lds(rw, (lds_void*)(slot + XB + (lw * 6 + j) * 1024), 16, vw,
                                               (k * 512 + cs * 64 + cc) * 1024 + n0 * 2, 0, LDAUX);
    }
  };
  const unsigned short pm = mean[n0 + lane], pv = var[n0 + lane], pg = g[n0 + lane], pb = bb[n0 + lane];
  SB();
  stage(0, 0);
  ST(1);
  stage(1, STAGE_B);
  ST(2);
  WAIT_V(10);
  ST(3);
  BARRIER();
  stage(2, 2 * STAGE_B);
  const float scale = bf2f(pg) * __builtin_amdgcn_rsqf(bf2f(pv) + 1e-5f);
  const float shift = bf2f(pb) - bf2f(pm) * scale;
  int slotoff = 3 * STAGE_B;
#pragma unroll 1
  for (int k = 1; k <= 5; ++k) {
    WAIT_V(10);
    BARRIER();
    stage(k + 2, slotoff);
    slotoff = (slotoff == 3 * STAGE_B) ? 0 : slotoff + STAGE_B;
  }
  ST(4);
  WAIT_V(10);
  BARRIER();
  if (lw == 0) {
    const unsigned a = (unsigned)(size_t)(lds_char*)shm + PARAM_OFF + lane * 4;
    asm volatile("ds_write_b32 %0, %1\n\tds_write_b32 %0, %2 offset:256" ::"v"(a), "v"(scale), "v"(shift) : "memory");
  }
  WAIT_V(0);
  WAIT_L0();
  ST(5);
  BARRIER();

  const __amdgpu_buffer_rsrc_t ro = __builtin_amdgcn_make_buffer_rsrc((void*)out, 0, 1024 * 4 * 512 * 2, SRD3);
  const unsigned sbase = (unsigned)(size_t)(lds_char*)shm + (lw * 8 + lr) * 128 + ((ls ^ lr) * 16);
  const int vo = (b0 + lw * 8 + lr) * 4096 + n0 * 2 + ls * 16;
#pragma unroll
  for (int ph = 0; ph < 2; ++ph) {
    BARRIER();
    const int sa = ph ? 1 : 0, sb = ph ? 2 : 3;
    u32x4 va, vb;
    asm volatile("ds_read_b128 %0, %2 offset:%3\n\tds_read_b128 %1, %2 offset:%4\n\ts_waitcnt lgkmcnt(0)"
                 : "=&v"(va), "=&v"(vb) : "v"(sbase), "n"(sa * 4096), "n"(sb * 4096) : "memory");
    __builtin_amdgcn_raw_buffer_store_b128(va, ro, vo, sa * 1024, STAUX);
    __builtin_amdgcn_raw_buffer_store_b128(vb, ro, vo, sb * 1024, STAUX);
    ST(6 + ph);
  }
  {
    const unsigned long long T3 = __builtin_amdgcn_s_memrealtime();
    const unsigned long long d = (PROBE == 1 ? 8ull : 0ull) * (T0b - T0a);
    while (__builtin_amdgcn_s_memrealtime() - T3 < d) {}
  }
}

extern "C" void kernel_launch(void* const* d_in, const int* in_sizes, int n_in, void* d_out, int out_size,
                              void* d_ws, size_t ws_size, hipStream_t stream) {
  conv_bn_silu<<<256, PF ? 576 : 512, 0, stream>>>((const char*)d_in[0], (const char*)d_in[1], (const unsigned short*)d_in[2],
                                        (const unsigned short*)d_in[3], (const unsigned short*)d_in[4],
                                        (const unsigned short*)d_in[5], (char*)d_out
                                        );
}
```

```cpp
#include <hip/hip_runtime.h>
#include <hip/hip_bf16.h>
#include <stdint.h>

typedef __attribute__((ext_vector_type(8))) __bf16 bf16x8;
typedef __attribute__((ext_vector_type(4))) short s16x4;
typedef __attribute__((ext_vector_type(8))) short s16x8;
typedef __attribute__((ext_vector_type(16))) float f32x16;
typedef __attribute__((ext_vector_type(4))) float f32x4;
typedef __attribute__((ext_vector_type(2))) float f32x2;
typedef __attribute__((ext_vector_type(4))) unsigned u32x4;
typedef __attribute__((ext_vector_type(2))) unsigned u32x2;
typedef __attribute__((address_space(3))) char lds_char;
typedef __attribute__((address_space(3))) void lds_void;

#define LDAUX 16
#define STAUX 16

constexpr int STAGE_B = 40960;
constexpr int XB = 16384;
constexpr int NSLOT = 4;
constexpr int PARAM_OFF = XB;
constexpr int SRD3 = 0x00020000;

#define WAIT_V(n) asm volatile("s_waitcnt vmcnt(%0)" ::"n"(n) : "memory")
#define WAIT_L0() asm volatile("s_waitcnt lgkmcnt(0)" ::: "memory")
#define BARRIER() do { asm volatile("" ::: "memory"); __builtin_amdgcn_s_barrier(); asm volatile("" ::: "memory"); } while (0)

#define ST(i)
#define STAMP_ARG
#define STAMP_PASS

__device__ __forceinline__ float bf2f(unsigned short v) { return __uint_as_float(((unsigned)v) << 16); }
__device__ __forceinline__ unsigned pack_bf16(float lo, float hi) {
  unsigned r;
  asm("v_cvt_pk_bf16_f32 %0, %1, %2" : "=v"(r) : "v"(lo), "v"(hi));
  return r;
}

struct Frags { s16x4 ul[3][2], uh[3][2]; s16x8 y[3][2]; };

#define RD128(dst, addr, off) asm volatile("ds_read_b128 %0, %1 offset:%2" : "=v"(dst) : "v"(addr), "n"(off))
#define RDTR(dst, addr, off) asm volatile("ds_read_b64_tr_b16 %0, %1 offset:%2" : "=v"(dst) : "v"(addr), "n"(off))
#define CAT(l, h) __builtin_bit_cast(bf16x8, __builtin_shufflevector(l, h, 0, 1, 2, 3, 4, 5, 6, 7))
#define BC(v) __builtin_bit_cast(bf16x8, v)
#define SB() __builtin_amdgcn_sched_barrier(0)

template <int SP>
struct Cfg {
  static constexpr int S0 = SP ? 3 : 0, S1 = SP ? 2 : 1, S2 = SP ? 1 : 2;
  static constexpr int K0 = SP ? 2 : 0, K1 = 1, K2 = SP ? 0 : 2;
  static constexpr int SA = SP ? 2 : 1, SBo = SP ? 3 : 0;
  static constexpr int S(int i) { return i == 0 ? S0 : i == 1 ? S1 : S2; }
  static constexpr int K(int i) { return i == 0 ? K0 : i == 1 ? K1 : K2; }
};

#define WOFF(K, T, E) (XB + (K) * 8192 + (T) * 4096 + (E) * 512)
#define MM(c, F, k, s, i, j) \
  asm volatile("v_mfma_f32_16x16x32_bf16 %0, %1, %2, %0" : "+v"(c[i][j]) : "v"(CAT(F.ul[k][i], F.uh[k][i])), "v"(BC(F.y[s][j])))
#define WM(n, c, F, k, s, i, j) \
  asm volatile("s_waitcnt lgkmcnt(%3)\n\tv_mfma_f32_16x16x32_bf16 %0, %1, %2, %0" : "+v"(c[i][j]) : "v"(CAT(F.ul[k][i], F.uh[k][i])), "v"(BC(F.y[s][j])), "n"(n))
#define RUL(N, k, i, T) RDTR(N.ul[k][i], aw[i], WOFF(G::K(k), T, 0))
#define RUH(N, k, i, T) RDTR(N.uh[k][i], aw[i], WOFF(G::K(k), T, 1))
#define RY(N, s, j, T) RD128(N.y[s][j], ax[T], G::S(s) * 4096 + (j) * 2048)
#define STEP(C, N, T, UPD)                                                          \
  do {                                                                              \
    WM(12, accA, C, 0, 0, 0, 0); SB();                                              \
    if ((T) == 0) { BARRIER(); SB(); }                                              \
    RUL(N, 0, 0, T); SB();                                                          \
    MM(accA, C, 0, 0, 0, 1); SB(); RUH(N, 0, 0, T); SB();                           \
    MM(accA, C, 0, 0, 1, 1); SB(); RY(N, 0, 0, T); SB();                            \
    MM(accA, C, 0, 0, 1, 0); SB(); RUL(N, 0, 1, T); SB();                           \
    WM(12, accB, C, 1, 0, 1, 0); SB(); RUH(N, 0, 1, T); SB();                       \
    MM(accB, C, 1, 0, 1, 1); SB(); RY(N, 0, 1, T); SB();                            \
    MM(accB, C, 1, 0, 0, 1); SB(); RUL(N, 1, 0, T); SB();                           \
    MM(accB, C, 1, 0, 0, 0); SB(); RUH(N, 1, 0, T); SB();                           \
    WM(14, accA, C, 1, 1, 0, 0); SB(); RUL(N, 1, 1, T); SB();                       \
    MM(accA, C, 1, 1, 0, 1); SB(); RUH(N, 1, 1, T); SB();                           \
    MM(accA, C, 1, 1, 1, 1); SB(); RY(N, 1, 0, T); SB();                            \
    MM(accA, C, 1, 1, 1, 0); SB(); RY(N, 1, 1, T); SB();                            \
    WM(14, accB, C, 2, 1, 1, 0); SB(); RUL(N, 2, 0, T); SB();                       \
    MM(accB, C, 2, 1, 1, 1); SB(); RUH(N, 2, 0, T); SB();                           \
    MM(accB, C, 2, 1, 0, 1); SB(); RUL(N, 2, 1, T); SB();                           \
    MM(accB, C, 2, 1, 0, 0); SB(); RUH(N, 2, 1, T); SB();                           \
    WM(15, accA, C, 2, 2, 0, 0); SB(); RY(N, 2, 0, T); SB();                        \
    MM(accA, C, 2, 2, 0, 1); SB(); RY(N, 2, 1, T); SB();                            \
    MM(accA, C, 2, 2, 1, 1); SB();                                                  \
    if (UPD) { aw[0] += delta; aw[1] += delta; SB(); }                              \
    MM(accA, C, 2, 2, 1, 0); SB();                                                  \
    if (UPD) { ax[0] += delta; ax[1] += delta; SB(); }                              \
  } while (0)

__device__ __forceinline__ void epi_acc(const f32x4 (&acc)[2][2], const f32x4 (&sc)[2], const f32x4 (&sh)[2], unsigned (&pk)[2][2][2]) {
  constexpr float L = -1.44269504088896340736f;
#pragma unroll
  for (int j = 0; j < 2; ++j)
#pragma unroll
    for (int i = 0; i < 2; ++i)
#pragma unroll
      for (int r = 0; r < 2; ++r) {
        const f32x2 a = {acc[i][j][2 * r], acc[i][j][2 * r + 1]};
        const f32x2 s = {sc[i][2 * r], sc[i][2 * r + 1]};
        const f32x2 f = {sh[i][2 * r], sh[i][2 * r + 1]};
        const f32x2 t = a * s + f;
        const f32x2 e = {__builtin_amdgcn_exp2f(t[0]), __builtin_amdgcn_exp2f(t[1])};
        const f32x2 d = e * L + L;
        const f32x2 q = {__builtin_amdgcn_rcpf(d[0]), __builtin_amdgcn_rcpf(d[1])};
        const f32x2 o = t * q;
        pk[i][j][r] = pack_bf16(o[0], o[1]);
      }
}

template <int SP>
__device__ __forceinline__ void consume(char* shm, int wid, int lane STAMP_ARG) {
  using G = Cfg<SP>;
  const int nh = wid & 1;
  const int g = lane >> 4, n = lane & 15;
  const unsigned lbase = (unsigned)(size_t)(lds_char*)shm;
  unsigned ax[2], aw[2];
  ax[0] = lbase + n * 128 + ((g ^ ((n >> 1) & 7)) * 16);
  ax[1] = ax[0] ^ 64;
  const int q = n >> 2, p = lane & 3;
  aw[0] = lbase + (8 * g + q) * 128 + (((4 * nh + (p >> 1)) ^ (((q >> 1) & 1) << 2) ^ ((g & 1) << 1)) * 16) + 8 * (p & 1);
  aw[1] = aw[0] ^ 32;
  unsigned apar = lbase + PARAM_OFF + (32 * nh + 4 * g) * 4;
  unsigned astg[2];
#pragma unroll
  for (int i = 0; i < 2; ++i) astg[i] = lbase + n * 128 + (((4 * nh + 2 * i + (g >> 1)) ^ (n & 7)) * 16) + 8 * (g & 1);

  f32x4 accA[2][2] = {}, accB[2][2] = {};
  Frags F0, F1;
  asm volatile("" : "+v"(accA[0][0]), "+v"(accA[0][1]), "+v"(accA[1][0]), "+v"(accA[1][1]), "+v"(accB[0][0]), "+v"(accB[0][1]),
               "+v"(accB[1][0]), "+v"(accB[1][1]), "+v"(ax[0]), "+v"(ax[1]), "+v"(aw[0]), "+v"(aw[1]), "+v"(apar), "+v"(astg[0]), "+v"(astg[1]));
  ST(1);
  BARRIER();
  ST(2);
  RUL(F0, 0, 0, 0); RUH(F0, 0, 0, 0); RY(F0, 0, 0, 0); RUL(F0, 0, 1, 0); RUH(F0, 0, 1, 0); RY(F0, 0, 1, 0);
  RUL(F0, 1, 0, 0); RUH(F0, 1, 0, 0); RUL(F0, 1, 1, 0); RUH(F0, 1, 1, 0); RY(F0, 1, 0, 0); RY(F0, 1, 1, 0);
  RUL(F0, 2, 0, 0); RUH(F0, 2, 0, 0); RUL(F0, 2, 1, 0); RUH(F0, 2, 1, 0); RY(F0, 2, 0, 0); RY(F0, 2, 1, 0);
  SB();

#pragma unroll 1
  for (int st = 0; st < 7; ++st) {
    const int delta = (st == 3) ? -3 * STAGE_B : STAGE_B;
    STEP(F0, F1, 1, 1);
    STEP(F1, F0, 0, 0);
  }
  {
    const int delta = 0;
    STEP(F0, F1, 1, 0);
  }

  f32x4 sc[2], sh[2];
  WM(12, accA, F1, 0, 0, 0, 0); SB(); RD128(sc[0], apar, 0); SB();
  MM(accA, F1, 0, 0, 0, 1); SB(); RD128(sh[0], apar, 256); SB();
  MM(accA, F1, 0, 0, 1, 1); SB(); RD128(sc[1], apar, 64); SB();
  MM(accA, F1, 0, 0, 1, 0); SB(); RD128(sh[1], apar, 320); SB();
  WM(12, accB, F1, 1, 0, 1, 0); SB();
  MM(accB, F1, 1, 0, 1, 1); SB();
  MM(accB, F1, 1, 0, 0, 1); SB();
  MM(accB, F1, 1, 0, 0, 0); SB();
  WM(10, accA, F1, 1, 1, 0, 0); SB();
  MM(accA, F1, 1, 1, 0, 1); SB();
  MM(accA, F1, 1, 1, 1, 1); SB();
  MM(accA, F1, 1, 1, 1, 0); SB();
  WM(6, accB, F1, 2, 1, 1, 0); SB();
  MM(accB, F1, 2, 1, 1, 1); SB();
  MM(accB, F1, 2, 1, 0, 1); SB();
  MM(accB, F1, 2, 1, 0, 0); SB();
  WM(4, accA, F1, 2, 2, 0, 0); SB();
  MM(accA, F1, 2, 2, 0, 1); SB();
  MM(accA, F1, 2, 2, 1, 1); SB();
  MM(accA, F1, 2, 2, 1, 0); SB();
  asm volatile("s_waitcnt lgkmcnt(0)\n\ts_nop 3"
               : "+v"(sc[0]), "+v"(sc[1]), "+v"(sh[0]), "+v"(sh[1]), "+v"(accB[0][0]), "+v"(accB[0][1]), "+v"(accB[1][0]), "+v"(accB[1][1]));
  SB();
  ST(3);

  unsigned pk[2][2][2];
  epi_acc(accB, sc, sh, pk);
#pragma unroll
  for (int j = 0; j < 2; ++j)
#pragma unroll
    for (int i = 0; i < 2; ++i) {
      const u32x2 v = {pk[i][j][0], pk[i][j][1]};
      asm volatile("ds_write_b64 %0, %1 offset:%2" ::"v"(astg[i]), "v"(v), "n"(G::SBo * 4096 + j * 2048) : "memory");
    }
  SB();
  asm volatile("s_nop 3" : "+v"(accA[0][0]), "+v"(accA[0][1]), "+v"(accA[1][0]), "+v"(accA[1][1]));
  WAIT_L0();
  BARRIER();
  SB();
  ST(4);
  epi_acc(accA, sc, sh, pk);
#pragma unroll
  for (int j = 0; j < 2; ++j)
#pragma unroll
    for (int i = 0; i < 2; ++i) {
      const u32x2 v = {pk[i][j][0], pk[i][j][1]};
      asm volatile("ds_write_b64 %0, %1 offset:%2" ::"v"(astg[i]), "v"(v), "n"(G::SA * 4096 + j * 2048) : "memory");
    }
  WAIT_L0();
  BARRIER();
  ST(5);
}

__global__ __launch_bounds__(512) void conv_bn_silu(
    const char* __restrict__ x, const char* __restrict__ w, const unsigned short* __restrict__ mean,
    const unsigned short* __restrict__ var, const unsigned short* __restrict__ g,
    const unsigned short* __restrict__ bb, char* __restrict__ out STAMP_ARG) {
  __shared__ __attribute__((aligned(1024))) char shm[NSLOT * STAGE_B];
  const int tid = threadIdx.x;
  const int wid = __builtin_amdgcn_readfirstlane(tid >> 6);
  const int lane = tid & 63;
  ST(0);

  if (wid < 4) {
    if (wid & 2) consume<1>(shm, wid, lane STAMP_PASS);
    else consume<0>(shm, wid, lane STAMP_PASS);
    return;
  }

  const int xcd = blockIdx.x & 7, idx = blockIdx.x >> 3;
  const int mt = (xcd >> 1) * 8 + (idx & 7);
  const int nt = (xcd & 1) * 4 + (idx >> 3);
  const int b0 = mt * 32, n0 = nt * 64;

  const int rot = 1 * ((blockIdx.x >> 1) & 1) + 7 * ((blockIdx.x >> 3) & 1) + 4 * ((blockIdx.x >> 4) & 1) + 4 * ((blockIdx.x >> 5) & 1) + 4 * ((blockIdx.x >> 7) & 1);
  const int lw = wid - 4;
  const __amdgpu_buffer_rsrc_t rx = __builtin_amdgcn_make_buffer_rsrc((void*)x, 0, 1024 * 4 * 512 * 2, SRD3);
  const __amdgpu_buffer_rsrc_t rw = __builtin_amdgcn_make_buffer_rsrc((void*)w, 0, 3 * 512 * 512 * 2, SRD3);
  const int lr = lane >> 3, ls = lane & 7;
  const int vx0 = lr * 4096 + ((ls ^ (lane >> 4)) * 16);
  const int vx1 = lr * 4096 + ((ls ^ (4 + (lane >> 4))) * 16);
  const int vw0 = lr * 1024 + ((ls ^ (((lane >> 4) & 1) << 2)) * 16);
  const int vw1 = vw0 ^ 32;
  const int sx = (b0 * 4 + lw) * 1024;
  auto stage = [&](int st, int slotoff) {
    const int cs = (st + rot) & 7;
    lds_char* slot = (lds_char*)shm + slotoff;
#pragma unroll
    for (int j = 0; j < 4; ++j)
      __builtin_amdgcn_raw_ptr_buffer_load_lds(rx, (lds_void*)(slot + (lw * 4 + j) * 1024), 16, (j & 1) ? vx1 : vx0,
                                               sx + j * 8 * 4096 + cs * 128, 0, LDAUX);
#pragma unroll
    for (int j = 0; j < 6; ++j) {
      const int r = lw * 48 + j * 8;
      const int k = r >> 6, cc = r & 63;
      __builtin_amdgcn_raw_ptr_buffer_load_lds(rw, (lds_void*)(slot + XB + (lw * 6 + j) * 1024), 16, (j & 1) ? vw1 : vw0,
                                               (k * 512 + cs * 64 + cc) * 1024 + n0 * 2, 0, LDAUX);
    }
  };
  const unsigned short pm = mean[n0 + lane], pv = var[n0 + lane], pg = g[n0 + lane], pb = bb[n0 + lane];
  SB();
  stage(0, 0);
  ST(1);
  stage(1, STAGE_B);
  ST(2);
  WAIT_V(10);
  ST(3);
  BARRIER();
  stage(2, 2 * STAGE_B);
  const float scale0 = bf2f(pg) * __builtin_amdgcn_rsqf(bf2f(pv) + 1e-5f);
  const float scale = scale0 * -1.44269504088896340736f;
  const float shift = (bf2f(pb) - bf2f(pm) * scale0) * -1.44269504088896340736f;
  int slotoff = 3 * STAGE_B;
#pragma unroll 1
  for (int k = 1; k <= 5; ++k) {
    WAIT_V(10);
    BARRIER();
    stage(k + 2, slotoff);
    slotoff = (slotoff == 3 * STAGE_B) ? 0 : slotoff + STAGE_B;
  }
  ST(4);
  WAIT_V(10);
  BARRIER();
  if (lw == 0) {
    const unsigned a = (unsigned)(size_t)(lds_char*)shm + PARAM_OFF + lane * 4;
    asm volatile("ds_write_b32 %0, %1\n\tds_write_b32 %0, %2 offset:256" ::"v"(a), "v"(scale), "v"(shift) : "memory");
  }
  WAIT_V(0);
  WAIT_L0();
  ST(5);
  BARRIER();

  const __amdgpu_buffer_rsrc_t ro = __builtin_amdgcn_make_buffer_rsrc((void*)out, 0, 1024 * 4 * 512 * 2, SRD3);
  const unsigned sbase = (unsigned)(size_t)(lds_char*)shm + (lw * 8 + lr) * 128 + ((ls ^ lr) * 16);
  const int vo = (b0 + lw * 8 + lr) * 4096 + n0 * 2 + ls * 16;
#pragma unroll
  for (int ph = 0; ph < 2; ++ph) {
    BARRIER();
    const int sa = ph ? 1 : 0, sb = ph ? 2 : 3;
    u32x4 va, vb;
    asm volatile("ds_read_b128 %0, %2 offset:%3\n\tds_read_b128 %1, %2 offset:%4\n\ts_waitcnt lgkmcnt(0)"
                 : "=&v"(va), "=&v"(vb) : "v"(sbase), "n"(sa * 4096), "n"(sb * 4096) : "memory");
    __builtin_amdgcn_raw_buffer_store_b128(va, ro, vo, sa * 1024, STAUX);
    __builtin_amdgcn_raw_buffer_store_b128(vb, ro, vo, sb * 1024, STAUX);
    ST(6 + ph);
  }
}

extern "C" void kernel_launch(void* const* d_in, const int* in_sizes, int n_in, void* d_out, int out_size,
                              void* d_ws, size_t ws_size, hipStream_t stream) {
  conv_bn_silu<<<256, 512, 0, stream>>>((const char*)d_in[0], (const char*)d_in[1], (const unsigned short*)d_in[2],
                                        (const unsigned short*)d_in[3], (const unsigned short*)d_in[4],
                                        (const unsigned short*)d_in[5], (char*)d_out
                                        );
}
```

```cpp
#include <hip/hip_runtime.h>
#include <hip/hip_bf16.h>
#include <stdint.h>

typedef __attribute__((ext_vector_type(8))) __bf16 bf16x8;
typedef __attribute__((ext_vector_type(4))) short s16x4;
typedef __attribute__((ext_vector_type(8))) short s16x8;
typedef __attribute__((ext_vector_type(16))) float f32x16;
typedef __attribute__((ext_vector_type(4))) float f32x4;
typedef __attribute__((ext_vector_type(2))) float f32x2;
typedef __attribute__((ext_vector_type(4))) unsigned u32x4;
typedef __attribute__((ext_vector_type(2))) unsigned u32x2;
typedef __attribute__((address_space(3))) char lds_char;
typedef __attribute__((address_space(3))) void lds_void;

#define LDAUX 16
#define STAUX 16

constexpr int STAGE_B = 40960;
constexpr int XB = 16384;
constexpr int NSLOT = 4;
constexpr int PARAM_OFF = XB;
constexpr int SRD3 = 0x00020000;

#define WAIT_V(n) asm volatile("s_waitcnt vmcnt(%0)" ::"n"(n) : "memory")
#define WAIT_L0() asm volatile("s_waitcnt lgkmcnt(0)" ::: "memory")
#define BARRIER() do { asm volatile("" ::: "memory"); __builtin_amdgcn_s_barrier(); asm volatile("" ::: "memory"); } while (0)

#define ST(i)
#define STAMP_ARG
#define STAMP_PASS

__device__ __forceinline__ float bf2f(unsigned short v) { return __uint_as_float(((unsigned)v) << 16); }
__device__ __forceinline__ unsigned pack_bf16(float lo, float hi) {
  unsigned r;
  asm("v_cvt_pk_bf16_f32 %0, %1, %2" : "=v"(r) : "v"(lo), "v"(hi));
  return r;
}

struct Frags { s16x4 ul[3][2], uh[3][2]; s16x8 y[4]; };

#define RD128(dst, addr, off) asm volatile("ds_read_b128 %0, %1 offset:%2" : "=v"(dst) : "v"(addr), "n"(off))
#define RDTR(dst, addr, off) asm volatile("ds_read_b64_tr_b16 %0, %1 offset:%2" : "=v"(dst) : "v"(addr), "n"(off))
#define CAT(l, h) __builtin_bit_cast(bf16x8, __builtin_shufflevector(l, h, 0, 1, 2, 3, 4, 5, 6, 7))
#define BC(v) __builtin_bit_cast(bf16x8, v)
#define SB() __builtin_amdgcn_sched_barrier(0)

#define WOFF(K, T, E) (XB + (K) * 8192 + (T) * 4096 + (E) * 512)
#define MM(F, k, i, s) \
  do { asm volatile("v_mfma_f32_16x16x32_bf16 %0, %1, %2, %0" : "+v"(acc[(s) - (k) + 1][i]) : "v"(CAT(F.ul[k][i], F.uh[k][i])), "v"(BC(F.y[s]))); SB(); } while (0)
#define WM(n, F, k, i, s) \
  do { asm volatile("s_waitcnt lgkmcnt(%3)\n\tv_mfma_f32_16x16x32_bf16 %0, %1, %2, %0" : "+v"(acc[(s) - (k) + 1][i]) : "v"(CAT(F.ul[k][i], F.uh[k][i])), "v"(BC(F.y[s])), "n"(n)); SB(); } while (0)
#define RUL(N, k, i, T) do { RDTR(N.ul[k][i], aw[i], WOFF(k, T, 0)); SB(); } while (0)
#define RUH(N, k, i, T) do { RDTR(N.uh[k][i], aw[i], WOFF(k, T, 1)); SB(); } while (0)
#define RY(N, s, T) do { RD128(N.y[s], ax[T], (s) * 4096); SB(); } while (0)
#define STEP(C, N, T, UPD)                                      \
  do {                                                          \
    WM(11, C, 0, 0, 0);                                         \
    if ((T) == 0) { BARRIER(); SB(); }                          \
    RUL(N, 0, 0, T);                                            \
    MM(C, 0, 0, 1); RUH(N, 0, 0, T);                            \
    MM(C, 0, 0, 2); RY(N, 0, T);                                \
    WM(10, C, 0, 1, 2); RY(N, 1, T);                            \
    MM(C, 0, 1, 1); RY(N, 2, T);                                \
    MM(C, 0, 1, 0); RUL(N, 0, 1, T);                            \
    MM(C, 1, 1, 0); RUH(N, 0, 1, T);                            \
    MM(C, 1, 1, 1); RUL(N, 1, 1, T);                            \
    MM(C, 1, 1, 2); RUH(N, 1, 1, T);                            \
    WM(13, C, 1, 1, 3); RY(N, 3, T);                            \
    MM(C, 1, 0, 3); RUL(N, 1, 0, T);                            \
    MM(C, 1, 0, 2); RUH(N, 1, 0, T);                            \
    MM(C, 1, 0, 0); RUL(N, 2, 0, T);                            \
    MM(C, 1, 0, 1); RUH(N, 2, 0, T);                            \
    WM(14, C, 2, 0, 1); RUL(N, 2, 1, T);                        \
    MM(C, 2, 0, 2); RUH(N, 2, 1, T);                            \
    MM(C, 2, 0, 3);                                             \
    MM(C, 2, 1, 3);                                             \
    if (UPD) { aw[0] += delta; aw[1] += delta; SB(); }          \
    MM(C, 2, 1, 2);                                             \
    if (UPD) { ax[0] += delta; ax[1] += delta; SB(); }          \
    MM(C, 2, 1, 1);                                             \
  } while (0)

struct Epi { f32x2 t[2], e[2]; u32x2 pk; };
constexpr float NLOG2E = -1.44269504088896340736f;
#define E_T(E, s, i)                                                                                             \
  do {                                                                                                           \
    E.t[0] = f32x2{acc[s][i][0], acc[s][i][1]} * f32x2{sc[i][0], sc[i][1]} + f32x2{sh[i][0], sh[i][1]}; SB();    \
    E.t[1] = f32x2{acc[s][i][2], acc[s][i][3]} * f32x2{sc[i][2], sc[i][3]} + f32x2{sh[i][2], sh[i][3]}; SB();    \
  } while (0)
#define E_X(E, r, h) do { E.e[r][h] = __builtin_amdgcn_exp2f(E.t[r][h]); SB(); } while (0)
#define E_D(E) do { E.e[0] = E.e[0] * NLOG2E + NLOG2E; SB(); E.e[1] = E.e[1] * NLOG2E + NLOG2E; SB(); } while (0)
#define E_R(E, r, h) do { E.e[r][h] = __builtin_amdgcn_rcpf(E.e[r][h]); SB(); } while (0)
#define E_O(E) do { E.t[0] = E.t[0] * E.e[0]; SB(); E.t[1] = E.t[1] * E.e[1]; SB(); } while (0)
#define E_P(E) do { E.pk[0] = pack_bf16(E.t[0][0], E.t[0][1]); SB(); E.pk[1] = pack_bf16(E.t[1][0], E.t[1][1]); SB(); } while (0)
#define E_W(E, s, i) do { asm volatile("ds_write_b64 %0, %1 offset:%2" ::"v"(astg[i]), "v"(E.pk), "n"((s) * 4096) : "memory"); SB(); } while (0)
#define E_HEAD(E, s, i) do { E_T(E, s, i); E_X(E, 0, 0); E_X(E, 0, 1); E_X(E, 1, 0); E_X(E, 1, 1); } while (0)
#define E_TAIL(E, s, i) do { E_D(E); E_R(E, 0, 0); E_R(E, 0, 1); E_R(E, 1, 0); E_R(E, 1, 1); E_O(E); E_P(E); E_W(E, s, i); } while (0)
#define NOP1() do { asm volatile("s_nop 1"); SB(); } while (0)

__device__ __forceinline__ void consume(char* shm, int wid, int lane STAMP_ARG) {
  const int nh = wid & 1, bh = wid >> 1;
  const int g = lane >> 4, n = lane & 15;
  const unsigned lbase = (unsigned)(size_t)(lds_char*)shm;
  unsigned ax[2], aw[2];
  ax[0] = lbase + bh * 2048 + n * 128 + ((g ^ ((n >> 1) & 7)) * 16);
  ax[1] = ax[0] ^ 64;
  const int q = n >> 2, p = lane & 3;
  aw[0] = lbase + (8 * g + q) * 128 + (((4 * nh + (p >> 1)) ^ (((q >> 1) & 1) << 2) ^ ((g & 1) << 1)) * 16) + 8 * (p & 1);
  aw[1] = aw[0] ^ 32;
  unsigned apar = lbase + PARAM_OFF + (32 * nh + 4 * g) * 4;
  unsigned astg[2];
#pragma unroll
  for (int i = 0; i < 2; ++i) astg[i] = lbase + bh * 2048 + n * 128 + (((4 * nh + 2 * i + (g >> 1)) ^ (n & 7)) * 16) + 8 * (g & 1);

  f32x4 acc[4][2] = {};
  Frags F0, F1;
  asm volatile("" : "+v"(acc[0][0]), "+v"(acc[0][1]), "+v"(acc[1][0]), "+v"(acc[1][1]), "+v"(acc[2][0]), "+v"(acc[2][1]),
               "+v"(acc[3][0]), "+v"(acc[3][1]), "+v"(ax[0]), "+v"(ax[1]), "+v"(aw[0]), "+v"(aw[1]), "+v"(apar), "+v"(astg[0]), "+v"(astg[1]));
  ST(1);
  BARRIER();
  ST(2);
  RUL(F0, 0, 0, 0); RUH(F0, 0, 0, 0); RY(F0, 0, 0); RY(F0, 1, 0); RY(F0, 2, 0); RUL(F0, 0, 1, 0); RUH(F0, 0, 1, 0);
  RUL(F0, 1, 1, 0); RUH(F0, 1, 1, 0); RY(F0, 3, 0); RUL(F0, 1, 0, 0); RUH(F0, 1, 0, 0);
  RUL(F0, 2, 0, 0); RUH(F0, 2, 0, 0); RUL(F0, 2, 1, 0); RUH(F0, 2, 1, 0);

#pragma unroll 1
  for (int st = 0; st < 7; ++st) {
    const int delta = (st == 3) ? -3 * STAGE_B : STAGE_B;
    STEP(F0, F1, 1, 1);
    STEP(F1, F0, 0, 0);
  }

  f32x4 sc[2], sh[2];
  WM(11, F0, 0, 0, 0); RUL(F1, 1, 0, 1);
  MM(F0, 0, 0, 1); RUH(F1, 1, 0, 1);
  MM(F0, 0, 0, 2); RY(F1, 0, 1);
  WM(10, F0, 0, 1, 2); RUL(F1, 2, 0, 1);
  MM(F0, 0, 1, 1); RUH(F1, 2, 0, 1);
  MM(F0, 0, 1, 0); RY(F1, 1, 1);
  MM(F0, 1, 1, 0); RUL(F1, 0, 0, 1);
  MM(F0, 1, 1, 1); RUH(F1, 0, 0, 1);
  MM(F0, 1, 1, 2); RY(F1, 2, 1);
  WM(13, F0, 1, 1, 3); RY(F1, 3, 1);
  MM(F0, 1, 0, 3); RD128(sc[0], apar, 0); SB();
  MM(F0, 1, 0, 2); RD128(sh[0], apar, 256); SB();
  MM(F0, 1, 0, 0); RUL(F1, 1, 1, 1);
  MM(F0, 1, 0, 1); RUH(F1, 1, 1, 1);
  WM(14, F0, 2, 0, 1); RUL(F1, 2, 1, 1);
  MM(F0, 2, 0, 2); RUH(F1, 2, 1, 1);
  MM(F0, 2, 0, 3); RUL(F1, 0, 1, 1);
  MM(F0, 2, 1, 3); RUH(F1, 0, 1, 1);
  MM(F0, 2, 1, 2); RD128(sc[1], apar, 64); SB();
  MM(F0, 2, 1, 1); RD128(sh[1], apar, 320); SB();

  Epi e0, e1, e2, e3;
  WM(14, F1, 1, 0, 0);
  MM(F1, 2, 0, 1); NOP1();
  WM(10, F1, 0, 0, 2); NOP1();
  MM(F1, 1, 0, 3); NOP1();
  asm volatile("s_waitcnt lgkmcnt(8)" : "+v"(sc[0]), "+v"(sh[0]), "+v"(acc[0][0])); SB();
  E_T(e0, 0, 0);
  WM(6, F1, 1, 1, 0); E_X(e0, 0, 0);
  WM(4, F1, 2, 1, 1); E_X(e0, 0, 1);
  WM(2, F1, 0, 1, 2); E_X(e0, 1, 0);
  MM(F1, 1, 1, 3); E_X(e0, 1, 1);
  MM(F1, 0, 0, 0); E_D(e0);
  MM(F1, 1, 0, 1); E_R(e0, 0, 0);
  MM(F1, 2, 0, 2); E_R(e0, 0, 1);
  MM(F1, 0, 0, 1); E_R(e0, 1, 0);
  MM(F1, 1, 0, 2); E_R(e0, 1, 1);
  MM(F1, 2, 0, 3); E_O(e0);
  asm volatile("s_waitcnt lgkmcnt(0)" : "+v"(sc[1]), "+v"(sh[1]), "+v"(acc[3][0])); SB();
  MM(F1, 0, 1, 0); E_P(e0);
  MM(F1, 1, 1, 1); E_W(e0, 0, 0); E_T(e1, 3, 0);
  MM(F1, 2, 1, 2); E_X(e1, 0, 0);
  MM(F1, 0, 1, 1); E_X(e1, 0, 1);
  MM(F1, 1, 1, 2); E_X(e1, 1, 0);
  MM(F1, 2, 1, 3); E_X(e1, 1, 1);
  asm volatile("" : "+v"(acc[0][1]), "+v"(acc[3][1])); SB();
  E_T(e2, 0, 1);
  E_TAIL(e1, 3, 0);
  E_X(e2, 0, 0); E_X(e2, 0, 1); E_X(e2, 1, 0); E_X(e2, 1, 1);
  E_T(e3, 3, 1);
  E_TAIL(e2, 0, 1);
  E_X(e3, 0, 0); E_X(e3, 0, 1); E_X(e3, 1, 0); E_X(e3, 1, 1);
  asm volatile("" : "+v"(acc[1][0]), "+v"(acc[2][0]), "+v"(acc[1][1]), "+v"(acc[2][1])); SB();
  E_T(e0, 1, 0);
  E_TAIL(e3, 3, 1);
  ST(3);
  E_X(e0, 0, 0); E_X(e0, 0, 1); E_X(e0, 1, 0); E_X(e0, 1, 1);
  E_T(e1, 2, 0);
  E_D(e0); E_R(e0, 0, 0); E_R(e0, 0, 1); E_R(e0, 1, 0); E_R(e0, 1, 1);
  WAIT_L0();
  BARRIER();
  SB();
  ST(4);
  E_O(e0); E_P(e0); E_W(e0, 1, 0);
  E_X(e1, 0, 0); E_X(e1, 0, 1); E_X(e1, 1, 0); E_X(e1, 1, 1);
  E_T(e2, 1, 1);
  E_TAIL(e1, 2, 0);
  E_X(e2, 0, 0); E_X(e2, 0, 1); E_X(e2, 1, 0); E_X(e2, 1, 1);
  E_T(e3, 2, 1);
  E_TAIL(e2, 1, 1);
  E_X(e3, 0, 0); E_X(e3, 0, 1); E_X(e3, 1, 0); E_X(e3, 1, 1);
  E_TAIL(e3, 2, 1);
  WAIT_L0();
  BARRIER();
  ST(5);
}

__global__ __launch_bounds__(512) void conv_bn_silu(
    const char* __restrict__ x, const char* __restrict__ w, const unsigned short* __restrict__ mean,
    const unsigned short* __restrict__ var, const unsigned short* __restrict__ g,
    const unsigned short* __restrict__ bb, char* __restrict__ out STAMP_ARG) {
  __shared__ __attribute__((aligned(1024))) char shm[NSLOT * STAGE_B];
  const int tid = threadIdx.x;
  const int wid = __builtin_amdgcn_readfirstlane(tid >> 6);
  const int lane = tid & 63;
  ST(0);

  if (wid < 4) {
    consume(shm, wid, lane STAMP_PASS);
    return;
  }

  const int xcd = blockIdx.x & 7, idx = blockIdx.x >> 3;
  const int mt = (xcd >> 1) * 8 + (idx & 7);
  const int nt = (xcd & 1) * 4 + (idx >> 3);
  const int b0 = mt * 32, n0 = nt * 64;

  const int rot = 1 * ((blockIdx.x >> 1) & 1) + 2 * ((blockIdx.x >> 3) & 1) + 4 * ((blockIdx.x >> 4) & 1) + 3 * ((blockIdx.x >> 6) & 1) + 5 * ((blockIdx.x >> 7) & 1);
  const int lw = wid - 4;
  const __amdgpu_buffer_rsrc_t rx = __builtin_amdgcn_make_buffer_rsrc((void*)x, 0, 1024 * 4 * 512 * 2, SRD3);
  const __amdgpu_buffer_rsrc_t rw = __builtin_amdgcn_make_buffer_rsrc((void*)w, 0, 3 * 512 * 512 * 2, SRD3);
  const int lr = lane >> 3, ls = lane & 7;
  const int vx0 = lr * 4096 + ((ls ^ (lane >> 4)) * 16);
  const int vx1 = lr * 4096 + ((ls ^ (4 + (lane >> 4))) * 16);
  const int vw0 = lr * 1024 + ((ls ^ (((lane >> 4) & 1) << 2)) * 16);
  const int vw1 = vw0 ^ 32;
  const int sx = (b0 * 4 + lw) * 1024;
  auto stage = [&](int st, int slotoff) {
    const int cs = (st + rot) & 7;
    lds_char* slot = (lds_char*)shm + slotoff;
#pragma unroll
    for (int j = 0; j < 4; ++j)
      __builtin_amdgcn_raw_ptr_buffer_load_lds(rx, (lds_void*)(slot + (lw * 4 + j) * 1024), 16, (j & 1) ? vx1 : vx0,
                                               sx + j * 8 * 4096 + cs * 128, 0, LDAUX);
#pragma unroll
    for (int j = 0; j < 6; ++j) {
      const int r = lw * 48 + j * 8;
      const int k = r >> 6, cc = r & 63;
      __builtin_amdgcn_raw_ptr_buffer_load_lds(rw, (lds_void*)(slot + XB + (lw * 6 + j) * 1024), 16, (j & 1) ? vw1 : vw0,
                                               (k * 512 + cs * 64 + cc) * 1024 + n0 * 2, 0, LDAUX);
    }
  };
  const unsigned short pm = mean[n0 + lane], pv = var[n0 + lane], pg = g[n0 + lane], pb = bb[n0 + lane];
  SB();
  stage(0, 0);
  ST(1);
  stage(1, STAGE_B);
  ST(2);
  WAIT_V(10);
  ST(3);
  BARRIER();
  stage(2, 2 * STAGE_B);
  const float scale0 = bf2f(pg) * __builtin_amdgcn_rsqf(bf2f(pv) + 1e-5f);
  const float scale = scale0 * -1.44269504088896340736f;
  const float shift = (bf2f(pb) - bf2f(pm) * scale0) * -1.44269504088896340736f;
  int slotoff = 3 * STAGE_B;
#pragma unroll 1
  for (int k = 1; k <= 5; ++k) {
    WAIT_V(10);
    BARRIER();
    stage(k + 2, slotoff);
    slotoff = (slotoff == 3 * STAGE_B) ? 0 : slotoff + STAGE_B;
  }
  ST(4);
  WAIT_V(10);
  BARRIER();
  if (lw == 0) {
    const unsigned a = (unsigned)(size_t)(lds_char*)shm + PARAM_OFF + lane * 4;
    asm volatile("ds_write_b32 %0, %1\n\tds_write_b32 %0, %2 offset:256" ::"v"(a), "v"(scale), "v"(shift) : "memory");
  }
  WAIT_V(0);
  WAIT_L0();
  ST(5);
  BARRIER();

  const __amdgpu_buffer_rsrc_t ro = __builtin_amdgcn_make_buffer_rsrc((void*)out, 0, 1024 * 4 * 512 * 2, SRD3);
  const unsigned sbase = (unsigned)(size_t)(lds_char*)shm + (lw * 8 + lr) * 128 + ((ls ^ lr) * 16);
  const int vo = (b0 + lw * 8 + lr) * 4096 + n0 * 2 + ls * 16;
#pragma unroll
  for (int ph = 0; ph < 2; ++ph) {
    BARRIER();
    const int sa = ph ? 1 : 0, sb = ph ? 2 : 3;
    u32x4 va, vb;
    asm volatile("ds_read_b128 %0, %2 offset:%3\n\tds_read_b128 %1, %2 offset:%4\n\ts_waitcnt lgkmcnt(0)"
                 : "=&v"(va), "=&v"(vb) : "v"(sbase), "n"(sa * 4096), "n"(sb * 4096) : "memory");
    __builtin_amdgcn_raw_buffer_store_b128(va, ro, vo, sa * 1024, STAUX);
    __builtin_amdgcn_raw_buffer_store_b128(vb, ro, vo, sb * 1024, STAUX);
    ST(6 + ph);
  }
}

extern "C" void kernel_launch(void* const* d_in, const int* in_sizes, int n_in, void* d_out, int out_size,
                              void* d_ws, size_t ws_size, hipStream_t stream) {
  conv_bn_silu<<<256, 512, 0, stream>>>((const char*)d_in[0], (const char*)d_in[1], (const unsigned short*)d_in[2],
                                        (const unsigned short*)d_in[3], (const unsigned short*)d_in[4],
                                        (const unsigned short*)d_in[5], (char*)d_out
                                        );
}
```

```cpp
#include <hip/hip_runtime.h>
#include <hip/hip_bf16.h>
#include <stdint.h>

typedef __attribute__((ext_vector_type(8))) __bf16 bf16x8;
typedef __attribute__((ext_vector_type(4))) short s16x4;
typedef __attribute__((ext_vector_type(8))) short s16x8;
typedef __attribute__((ext_vector_type(16))) float f32x16;
typedef __attribute__((ext_vector_type(4))) float f32x4;
typedef __attribute__((ext_vector_type(2))) float f32x2;
typedef __attribute__((ext_vector_type(4))) unsigned u32x4;
typedef __attribute__((ext_vector_type(2))) unsigned u32x2;
typedef __attribute__((address_space(3))) char lds_char;
typedef __attribute__((address_space(3))) void lds_void;

#define LDAUX 16
#define STAUX 16

constexpr int STAGE_B = 40960;
constexpr int XB = 16384;
constexpr int NSLOT = 4;
constexpr int PARAM_OFF = XB;
constexpr int SRD3 = 0x00020000;

#define WAIT_V(n) asm volatile("s_waitcnt vmcnt(%0)" ::"n"(n) : "memory")
#define WAIT_L0() asm volatile("s_waitcnt lgkmcnt(0)" ::: "memory")
#define BARRIER() do { asm volatile("" ::: "memory"); __builtin_amdgcn_s_barrier(); asm volatile("" ::: "memory"); } while (0)

#define ST(i)
#define STAMP_ARG
#define STAMP_PASS

__device__ __forceinline__ float bf2f(unsigned short v) { return __uint_as_float(((unsigned)v) << 16); }
__device__ __forceinline__ unsigned pack_bf16(float lo, float hi) {
  typedef __attribute__((ext_vector_type(2))) __bf16 bf16x2;
  const f32x2 v = {lo, hi};
  return __builtin_bit_cast(unsigned, __builtin_convertvector(v, bf16x2));
}

struct Frags { s16x4 ul[3][2], uh[3][2]; s16x8 y[4]; };

#define RD128(dst, addr, off) asm volatile("ds_read_b128 %0, %1 offset:%2" : "=v"(dst) : "v"(addr), "n"(off))
#define RDTR(dst, addr, off) asm volatile("ds_read_b64_tr_b16 %0, %1 offset:%2" : "=v"(dst) : "v"(addr), "n"(off))
#define CAT(l, h) __builtin_bit_cast(bf16x8, __builtin_shufflevector(l, h, 0, 1, 2, 3, 4, 5, 6, 7))
#define BC(v) __builtin_bit_cast(bf16x8, v)
#define SB() __builtin_amdgcn_sched_barrier(0)

#define WOFF(K, T, E) (XB + (K) * 8192 + (T) * 4096 + (E) * 512)
#define MM(F, k, i, s) \
  do { asm volatile("v_mfma_f32_16x16x32_bf16 %0, %1, %2, %0" : "+v"(acc[(s) - (k) + 1][i]) : "v"(CAT(F.ul[k][i], F.uh[k][i])), "v"(BC(F.y[s]))); SB(); } while (0)
#define WM(n, F, k, i, s) \
  do { asm volatile("s_waitcnt lgkmcnt(%3)\n\tv_mfma_f32_16x16x32_bf16 %0, %1, %2, %0" : "+v"(acc[(s) - (k) + 1][i]) : "v"(CAT(F.ul[k][i], F.uh[k][i])), "v"(BC(F.y[s])), "n"(n)); SB(); } while (0)
#define RUL(N, k, i, T) do { RDTR(N.ul[k][i], aw[i], WOFF(k, T, 0)); SB(); } while (0)
#define RUH(N, k, i, T) do { RDTR(N.uh[k][i], aw[i], WOFF(k, T, 1)); SB(); } while (0)
#define RY(N, s, T) do { RD128(N.y[s], ax[T], (s) * 4096); SB(); } while (0)
#define STEP(C, N, T, UPD)                                      \
  do {                                                          \
    WM(13, C, 0, 0, 0);                                         \
    if ((T) == 0) { BARRIER(); SB(); }                          \
    RUL(N, 0, 0, T);                                            \
    WM(13, C, 0, 0, 1); RUH(N, 0, 0, T);                        \
    WM(13, C, 0, 0, 2); RY(N, 0, T);                            \
    WM(10, C, 0, 1, 2); RY(N, 1, T);                            \
    MM(C, 0, 1, 1); RY(N, 2, T);                                \
    MM(C, 0, 1, 0); RUL(N, 0, 1, T);                            \
    MM(C, 1, 1, 0); RUH(N, 0, 1, T);                            \
    MM(C, 1, 1, 1); RUL(N, 1, 1, T);                            \
    MM(C, 1, 1, 2); RUH(N, 1, 1, T);                            \
    WM(13, C, 1, 1, 3); RY(N, 3, T);                            \
    MM(C, 1, 0, 3); RUL(N, 1, 0, T);                            \
    MM(C, 1, 0, 2); RUH(N, 1, 0, T);                            \
    MM(C, 1, 0, 0); RUL(N, 2, 0, T);                            \
    MM(C, 1, 0, 1); RUH(N, 2, 0, T);                            \
    WM(14, C, 2, 0, 1); RUL(N, 2, 1, T);                        \
    MM(C, 2, 0, 2); RUH(N, 2, 1, T);                            \
    MM(C, 2, 0, 3);                                             \
    MM(C, 2, 1, 3);                                             \
    if (UPD) { aw[0] += delta; aw[1] += delta; SB(); }          \
    MM(C, 2, 1, 2);                                             \
    if (UPD) { ax[0] += delta; ax[1] += delta; SB(); }          \
    MM(C, 2, 1, 1);                                             \
  } while (0)

struct Epi { f32x2 t[2], e[2]; u32x2 pk; };
constexpr float NLOG2E = -1.44269504088896340736f;
#define E_T(E, s, i)                                                                                             \
  do {                                                                                                           \
    E.t[0] = f32x2{acc[s][i][0], acc[s][i][1]} * f32x2{sc[i][0], sc[i][1]} + f32x2{sh[i][0], sh[i][1]}; SB();    \
    E.t[1] = f32x2{acc[s][i][2], acc[s][i][3]} * f32x2{sc[i][2], sc[i][3]} + f32x2{sh[i][2], sh[i][3]}; SB();    \
  } while (0)
#define E_X(E, r, h) do { E.e[r][h] = __builtin_amdgcn_exp2f(E.t[r][h]); SB(); } while (0)
#define E_D(E) do { E.e[0] = E.e[0] * NLOG2E + NLOG2E; SB(); E.e[1] = E.e[1] * NLOG2E + NLOG2E; SB(); } while (0)
#define E_R(E, r, h) do { E.e[r][h] = __builtin_amdgcn_rcpf(E.e[r][h]); SB(); } while (0)
#define E_O(E) do { E.t[0] = E.t[0] * E.e[0]; SB(); E.t[1] = E.t[1] * E.e[1]; SB(); } while (0)
#define E_P(E) do { E.pk[0] = pack_bf16(E.t[0][0], E.t[0][1]); SB(); E.pk[1] = pack_bf16(E.t[1][0], E.t[1][1]); SB(); } while (0)
#define E_W(E, s, i) do { asm volatile("ds_write_b64 %0, %1 offset:%2" ::"v"(astg[i]), "v"(E.pk), "n"((s) * 4096) : "memory"); SB(); } while (0)
#define E_HEAD(E, s, i) do { E_T(E, s, i); E_X(E, 0, 0); E_X(E, 0, 1); E_X(E, 1, 0); E_X(E, 1, 1); } while (0)
#define E_TAIL(E, s, i) do { E_D(E); E_R(E, 0, 0); E_R(E, 0, 1); E_R(E, 1, 0); E_R(E, 1, 1); E_O(E); E_P(E); E_W(E, s, i); } while (0)
#define NOP1() do { asm volatile("s_nop 1"); SB(); } while (0)

__device__ __forceinline__ void consume(char* shm, int wid, int lane STAMP_ARG) {
  const int nh = wid & 1, bh = wid >> 1;
  const int g = lane >> 4, n = lane & 15;
  const unsigned lbase = (unsigned)(size_t)(lds_char*)shm;
  unsigned ax[2], aw[2];
  ax[0] = lbase + bh * 2048 + n * 128 + ((g ^ ((n >> 1) & 7)) * 16);
  ax[1] = ax[0] ^ 64;
  const int q = n >> 2, p = lane & 3;
  aw[0] = lbase + (8 * g + q) * 128 + (((4 * nh + (p >> 1)) ^ (((q >> 1) & 1) << 2) ^ ((g & 1) << 1)) * 16) + 8 * (p & 1);
  aw[1] = aw[0] ^ 32;
  unsigned apar = lbase + PARAM_OFF + (32 * nh + 4 * g) * 4;
  unsigned astg[2];
#pragma unroll
  for (int i = 0; i < 2; ++i) astg[i] = lbase + bh * 2048 + n * 128 + (((4 * nh + 2 * i + (g >> 1)) ^ (n & 7)) * 16) + 8 * (g & 1);

  f32x4 acc[4][2] = {};
  Frags F0, F1;
  asm volatile("" : "+v"(acc[0][0]), "+v"(acc[0][1]), "+v"(acc[1][0]), "+v"(acc[1][1]), "+v"(acc[2][0]), "+v"(acc[2][1]),
               "+v"(acc[3][0]), "+v"(acc[3][1]), "+v"(ax[0]), "+v"(ax[1]), "+v"(aw[0]), "+v"(aw[1]), "+v"(apar), "+v"(astg[0]), "+v"(astg[1]));
  ST(1);
  BARRIER();
  ST(2);
  RUL(F0, 0, 0, 0); RUH(F0, 0, 0, 0); RY(F0, 0, 0); RY(F0, 1, 0); RY(F0, 2, 0); RUL(F0, 0, 1, 0); RUH(F0, 0, 1, 0);
  RUL(F0, 1, 1, 0); RUH(F0, 1, 1, 0); RY(F0, 3, 0); RUL(F0, 1, 0, 0); RUH(F0, 1, 0, 0);
  RUL(F0, 2, 0, 0); RUH(F0, 2, 0, 0); RUL(F0, 2, 1, 0); RUH(F0, 2, 1, 0);

#pragma unroll 1
  for (int st = 0; st < 6; ++st) {
    const int delta = (st == 3) ? -3 * STAGE_B : STAGE_B;
    STEP(F0, F1, 1, 1);
    STEP(F1, F0, 0, 0);
  }
  {
    const int delta = STAGE_B;
    STEP(F0, F1, 1, 1);
  }

  f32x4 sc[2], sh[2];
  Frags G;
#define RGY(s) RY(G, s, 1)
#define GM(k, i, s) \
  do { asm volatile("v_mfma_f32_16x16x32_bf16 %0, %1, %2, %0" : "+v"(acc[(s) - (k) + 1][i]) : "v"(CAT(F1.ul[k][i], F1.uh[k][i])), "v"(BC(G.y[s]))); SB(); } while (0)
#define WGM(n, k, i, s) \
  do { asm volatile("s_waitcnt lgkmcnt(%3)\n\tv_mfma_f32_16x16x32_bf16 %0, %1, %2, %0" : "+v"(acc[(s) - (k) + 1][i]) : "v"(CAT(F1.ul[k][i], F1.uh[k][i])), "v"(BC(G.y[s])), "n"(n)); SB(); } while (0)
  WM(13, F1, 0, 0, 0); BARRIER(); SB(); RUL(F0, 1, 0, 0);
  WM(13, F1, 0, 0, 1); RUH(F0, 1, 0, 0);
  WM(13, F1, 0, 0, 2); RY(F0, 0, 0);
  WM(7, F1, 1, 0, 0); RUL(F0, 2, 0, 0); RD128(sc[0], apar, 0); SB();
  MM(F1, 1, 0, 1); RUH(F0, 2, 0, 0); RD128(sh[0], apar, 256); SB();
  MM(F1, 1, 0, 2); RY(F0, 1, 0); RUL(F0, 0, 1, 0);
  MM(F1, 1, 0, 3); RGY(0);
  WM(10, F1, 2, 0, 1); RGY(1); RUH(F0, 0, 1, 0);
  MM(F1, 2, 0, 2); RUL(F1, 1, 0, 1);
  MM(F1, 2, 0, 3); RUH(F1, 1, 0, 1);
  MM(F1, 0, 1, 0); RUL(F0, 0, 0, 0);
  MM(F1, 0, 1, 1); RUH(F0, 0, 0, 0);
  MM(F1, 0, 1, 2); RUL(F1, 2, 0, 1);
  MM(F1, 1, 1, 0); RUH(F1, 2, 0, 1);
  MM(F1, 1, 1, 1); RY(F0, 2, 0);
  MM(F1, 1, 1, 2); RY(F0, 3, 0);
  MM(F1, 1, 1, 3); RUL(F1, 0, 0, 1);
  MM(F1, 2, 1, 1); RUH(F1, 0, 0, 1);
  MM(F1, 2, 1, 2); RGY(2);
  MM(F1, 2, 1, 3); RGY(3);

  Epi e0, e1, e2, e3;
  WM(10, F0, 1, 0, 0); RUL(F0, 1, 1, 0); RUH(F0, 1, 1, 0);
  MM(F0, 2, 0, 1); RUL(F0, 2, 1, 0); RUH(F0, 2, 1, 0);
  GM(1, 0, 0); RUL(F1, 0, 1, 1);
  WGM(9, 2, 0, 1); RUH(F1, 0, 1, 1); RUL(F1, 1, 1, 1);
  MM(F0, 0, 0, 2); RUH(F1, 1, 1, 1); RUL(F1, 2, 1, 1);
  MM(F0, 1, 0, 3); RUH(F1, 2, 1, 1);
  WGM(10, 0, 0, 2);
  asm volatile("" : "+v"(sc[0]), "+v"(sh[0]), "+v"(acc[0][0])); SB();
  E_T(e0, 0, 0);
  GM(1, 0, 3); E_X(e0, 0, 0);
  MM(F0, 0, 0, 0); E_X(e0, 0, 1);
  MM(F0, 1, 0, 1); E_X(e0, 1, 0);
  MM(F0, 2, 0, 2); E_X(e0, 1, 1);
  GM(0, 0, 0); E_D(e0);
  GM(1, 0, 1); E_R(e0, 0, 0);
  GM(2, 0, 2); E_R(e0, 0, 1);
  MM(F0, 0, 0, 1); E_R(e0, 1, 0);
  MM(F0, 1, 0, 2); E_R(e0, 1, 1);
  MM(F0, 2, 0, 3); E_O(e0);
  GM(0, 0, 1); E_P(e0);
  GM(1, 0, 2); E_W(e0, 0, 0);
  asm volatile("s_waitcnt lgkmcnt(1)" : "+v"(acc[3][0])); SB();
  GM(2, 0, 3); E_T(e1, 3, 0);
  MM(F0, 1, 1, 0); E_X(e1, 0, 0); RD128(sc[1], apar, 64); SB();
  MM(F0, 2, 1, 1); E_X(e1, 0, 1); RD128(sh[1], apar, 320); SB();
  GM(1, 1, 0); E_X(e1, 1, 0);
  GM(2, 1, 1); E_X(e1, 1, 1);
  MM(F0, 0, 1, 2); E_D(e1);
  MM(F0, 1, 1, 3); E_R(e1, 0, 0);
  GM(0, 1, 2); E_R(e1, 0, 1);
  GM(1, 1, 3); E_R(e1, 1, 0);
  MM(F0, 0, 1, 0); E_R(e1, 1, 1);
  MM(F0, 1, 1, 1); E_O(e1);
  MM(F0, 2, 1, 2); E_P(e1);
  GM(0, 1, 0); E_W(e1, 3, 0);
  asm volatile("s_waitcnt lgkmcnt(1)" : "+v"(sc[1]), "+v"(sh[1]), "+v"(acc[0][1])); SB();
  GM(1, 1, 1); E_T(e2, 0, 1);
  GM(2, 1, 2); E_X(e2, 0, 0);
  MM(F0, 0, 1, 1); E_X(e2, 0, 1);
  MM(F0, 1, 1, 2); E_X(e2, 1, 0);
  MM(F0, 2, 1, 3); E_X(e2, 1, 1);
  GM(0, 1, 1); E_D(e2);
  GM(1, 1, 2); E_R(e2, 0, 0);
  GM(2, 1, 3);
  asm volatile("" : "+v"(acc[3][1])); SB();
  E_R(e2, 0, 1); E_R(e2, 1, 0); E_R(e2, 1, 1);
  E_T(e3, 3, 1);
  E_O(e2); E_P(e2); E_W(e2, 0, 1);
  E_X(e3, 0, 0); E_X(e3, 0, 1); E_X(e3, 1, 0); E_X(e3, 1, 1);
  asm volatile("" : "+v"(acc[1][0]), "+v"(acc[2][0]), "+v"(acc[1][1]), "+v"(acc[2][1])); SB();
  E_T(e0, 1, 0);
  E_TAIL(e3, 3, 1);
  ST(3);
  E_X(e0, 0, 0); E_X(e0, 0, 1); E_X(e0, 1, 0); E_X(e0, 1, 1);
  E_T(e1, 2, 0);
  E_D(e0); E_R(e0, 0, 0); E_R(e0, 0, 1); E_R(e0, 1, 0); E_R(e0, 1, 1);
  WAIT_L0();
  BARRIER();
  SB();
  ST(4);
  E_O(e0); E_P(e0); E_W(e0, 1, 0);
  E_X(e1, 0, 0); E_X(e1, 0, 1); E_X(e1, 1, 0); E_X(e1, 1, 1);
  E_T(e2, 1, 1);
  E_TAIL(e1, 2, 0);
  E_X(e2, 0, 0); E_X(e2, 0, 1); E_X(e2, 1, 0); E_X(e2, 1, 1);
  E_T(e3, 2, 1);
  E_TAIL(e2, 1, 1);
  E_X(e3, 0, 0); E_X(e3, 0, 1); E_X(e3, 1, 0); E_X(e3, 1, 1);
  E_TAIL(e3, 2, 1);
  WAIT_L0();
  BARRIER();
  ST(5);
}

__global__ __launch_bounds__(512) void conv_bn_silu(
    const char* __restrict__ x, const char* __restrict__ w, const unsigned short* __restrict__ mean,
    const unsigned short* __restrict__ var, const unsigned short* __restrict__ g,
    const unsigned short* __restrict__ bb, char* __restrict__ out STAMP_ARG) {
  __shared__ __attribute__((aligned(1024))) char shm[NSLOT * STAGE_B];
  const int tid = threadIdx.x;
  const int wid = __builtin_amdgcn_readfirstlane(tid >> 6);
  const int lane = tid & 63;
  ST(0);

  if (wid < 4) {
    consume(shm, wid, lane STAMP_PASS);
    return;
  }

  const int xcd = blockIdx.x & 7, idx = blockIdx.x >> 3;
  const int mt = (xcd >> 1) * 8 + (idx & 7);
  const int nt = (xcd & 1) * 4 + (idx >> 3);
  const int b0 = mt * 32, n0 = nt * 64;

  const int rot = 1 * ((blockIdx.x >> 1) & 1) + 2 * ((blockIdx.x >> 3) & 1) + 4 * ((blockIdx.x >> 4) & 1) + 3 * ((blockIdx.x >> 6) & 1) + 5 * ((blockIdx.x >> 7) & 1);
  const int lw = wid - 4;
  const __amdgpu_buffer_rsrc_t rx = __builtin_amdgcn_make_buffer_rsrc((void*)x, 0, 1024 * 4 * 512 * 2, SRD3);
  const __amdgpu_buffer_rsrc_t rw = __builtin_amdgcn_make_buffer_rsrc((void*)w, 0, 3 * 512 * 512 * 2, SRD3);
  const int lr = lane >> 3, ls = lane & 7;
  const int vx0 = lr * 4096 + ((ls ^ (lane >> 4)) * 16);
  const int vx1 = lr * 4096 + ((ls ^ (4 + (lane >> 4))) * 16);
  const int vw0 = lr * 1024 + ((ls ^ (((lane >> 4) & 1) << 2)) * 16);
  const int vw1 = vw0 ^ 32;
  const int sx = (b0 * 4 + lw) * 1024;
  auto stage = [&](int st, int slotoff) {
    const int cs = (st + rot) & 7;
    lds_char* slot = (lds_char*)shm + slotoff;
#pragma unroll
    for (int j = 0; j < 4; ++j)
      __builtin_amdgcn_raw_ptr_buffer_load_lds(rx, (lds_void*)(slot + (lw * 4 + j) * 1024), 16, (j & 1) ? vx1 : vx0,
                                               sx + j * 8 * 4096 + cs * 128, 0, LDAUX);
#pragma unroll
    for (int j = 0; j < 6; ++j) {
      const int r = lw * 48 + j * 8;
      const int k = r >> 6, cc = r & 63;
      __builtin_amdgcn_raw_ptr_buffer_load_lds(rw, (lds_void*)(slot + XB + (lw * 6 + j) * 1024), 16, (j & 1) ? vw1 : vw0,
                                               (k * 512 + cs * 64 + cc) * 1024 + n0 * 2, 0, LDAUX);
    }
  };
  const unsigned short pm = mean[n0 + lane], pv = var[n0 + lane], pg = g[n0 + lane], pb = bb[n0 + lane];
  SB();
  stage(0, 0);
  ST(1);
  stage(1, STAGE_B);
  ST(2);
  WAIT_V(10);
  ST(3);
  BARRIER();
  stage(2, 2 * STAGE_B);
  const float scale0 = bf2f(pg) * __builtin_amdgcn_rsqf(bf2f(pv) + 1e-5f);
  const float scale = scale0 * -1.44269504088896340736f;
  const float shift = (bf2f(pb) - bf2f(pm) * scale0) * -1.44269504088896340736f;
  int slotoff = 3 * STAGE_B;
#pragma unroll 1
  for (int k = 1; k <= 5; ++k) {
    WAIT_V(10);
    BARRIER();
    stage(k + 2, slotoff);
    slotoff = (slotoff == 3 * STAGE_B) ? 0 : slotoff + STAGE_B;
  }
  ST(4);
  WAIT_V(10);
  BARRIER();
  if (lw == 0) {
    const unsigned a = (unsigned)(size_t)(lds_char*)shm + PARAM_OFF + lane * 4;
    asm volatile("ds_write_b32 %0, %1\n\tds_write_b32 %0, %2 offset:256" ::"v"(a), "v"(scale), "v"(shift) : "memory");
  }
  WAIT_V(0);
  WAIT_L0();
  ST(5);
  BARRIER();

  const __amdgpu_buffer_rsrc_t ro = __builtin_amdgcn_make_buffer_rsrc((void*)out, 0, 1024 * 4 * 512 * 2, SRD3);
  const unsigned sbase = (unsigned)(size_t)(lds_char*)shm + (lw * 8 + lr) * 128 + ((ls ^ lr) * 16);
  const int vo = (b0 + lw * 8 + lr) * 4096 + n0 * 2 + ls * 16;
#pragma unroll
  for (int ph = 0; ph < 2; ++ph) {
    BARRIER();
    const int sa = ph ? 1 : 0, sb = ph ? 2 : 3;
    u32x4 va, vb;
    asm volatile("ds_read_b128 %0, %2 offset:%3\n\tds_read_b128 %1, %2 offset:%4\n\ts_waitcnt lgkmcnt(0)"
                 : "=&v"(va), "=&v"(vb) : "v"(sbase), "n"(sa * 4096), "n"(sb * 4096) : "memory");
    __builtin_amdgcn_raw_buffer_store_b128(va, ro, vo, sa * 1024, STAUX);
    __builtin_amdgcn_raw_buffer_store_b128(vb, ro, vo, sb * 1024, STAUX);
    ST(6 + ph);
  }
}

extern "C" void kernel_launch(void* const* d_in, const int* in_sizes, int n_in, void* d_out, int out_size,
                              void* d_ws, size_t ws_size, hipStream_t stream) {
  conv_bn_silu<<<256, 512, 0, stream>>>((const char*)d_in[0], (const char*)d_in[1], (const unsigned short*)d_in[2],
                                        (const unsigned short*)d_in[3], (const unsigned short*)d_in[4],
                                        (const unsigned short*)d_in[5], (char*)d_out
                                        );
}
```

```cpp
#include <hip/hip_runtime.h>
#include <hip/hip_bf16.h>
#include <stdint.h>

typedef __attribute__((ext_vector_type(8))) __bf16 bf16x8;
typedef __attribute__((ext_vector_type(4))) short s16x4;
typedef __attribute__((ext_vector_type(8))) short s16x8;
typedef __attribute__((ext_vector_type(16))) float f32x16;
typedef __attribute__((ext_vector_type(4))) float f32x4;
typedef __attribute__((ext_vector_type(2))) float f32x2;
typedef __attribute__((ext_vector_type(4))) unsigned u32x4;
typedef __attribute__((ext_vector_type(2))) unsigned u32x2;
typedef __attribute__((address_space(3))) char lds_char;
typedef __attribute__((address_space(3))) void lds_void;

#define LDAUX 16
#define STAUX 16

constexpr int STAGE_B = 40960;
constexpr int XB = 16384;
constexpr int NSLOT = 4;
constexpr int PARAM_OFF = XB;
constexpr int SRD3 = 0x00020000;

#define WAIT_V(n) asm volatile("s_waitcnt vmcnt(%0)" ::"n"(n) : "memory")
#define WAIT_L0() asm volatile("s_waitcnt lgkmcnt(0)" ::: "memory")
#define BARRIER() do { asm volatile("" ::: "memory"); __builtin_amdgcn_s_barrier(); asm volatile("" ::: "memory"); } while (0)

__device__ __forceinline__ float bf2f(unsigned short v) { return __uint_as_float(((unsigned)v) << 16); }
__device__ __forceinline__ unsigned pack_bf16(float lo, float hi) {
  typedef __attribute__((ext_vector_type(2))) __bf16 bf16x2;
  const f32x2 v = {lo, hi};
  return __builtin_bit_cast(unsigned, __builtin_convertvector(v, bf16x2));
}

struct Frags { s16x4 ul[3][2], uh[3][2]; s16x8 y[4]; };

#define RD128(dst, addr, off) asm volatile("ds_read_b128 %0, %1 offset:%2" : "=v"(dst) : "v"(addr), "n"(off))
#define RDTR(dst, addr, off) asm volatile("ds_read_b64_tr_b16 %0, %1 offset:%2" : "=v"(dst) : "v"(addr), "n"(off))
#define CAT(l, h) __builtin_bit_cast(bf16x8, __builtin_shufflevector(l, h, 0, 1, 2, 3, 4, 5, 6, 7))
#define BC(v) __builtin_bit_cast(bf16x8, v)
#define SB() __builtin_amdgcn_sched_barrier(0)

#define WOFF(K, T, E) (XB + (K) * 8192 + (T) * 4096 + (E) * 512)
#define MM(F, k, i, s) \
  do { asm volatile("v_mfma_f32_16x16x32_bf16 %0, %1, %2, %0" : "+v"(acc[(s) - (k) + 1][i]) : "v"(CAT(F.ul[k][i], F.uh[k][i])), "v"(BC(F.y[s]))); SB(); } while (0)
#define WM(n, F, k, i, s) \
  do { asm volatile("s_waitcnt lgkmcnt(%3)\n\tv_mfma_f32_16x16x32_bf16 %0, %1, %2, %0" : "+v"(acc[(s) - (k) + 1][i]) : "v"(CAT(F.ul[k][i], F.uh[k][i])), "v"(BC(F.y[s])), "n"(n)); SB(); } while (0)
#define RUL(N, k, i, T) do { RDTR(N.ul[k][i], aw[i], WOFF(k, T, 0)); SB(); } while (0)
#define RUH(N, k, i, T) do { RDTR(N.uh[k][i], aw[i], WOFF(k, T, 1)); SB(); } while (0)
#define RY(N, s, T) do { RD128(N.y[s], ax[T], (s) * 4096); SB(); } while (0)
#define STEP(C, N, T, UPD)                                      \
  do {                                                          \
    WM(13, C, 0, 0, 0);                                         \
    if ((T) == 0) { BARRIER(); SB(); }                          \
    RUL(N, 0, 0, T);                                            \
    WM(13, C, 0, 0, 1); RUH(N, 0, 0, T);                        \
    WM(13, C, 0, 0, 2); RY(N, 0, T);                            \
    WM(10, C, 0, 1, 2); RY(N, 1, T);                            \
    MM(C, 0, 1, 1); RY(N, 2, T);                                \
    MM(C, 0, 1, 0); RUL(N, 0, 1, T);                            \
    MM(C, 1, 1, 0); RUH(N, 0, 1, T);                            \
    MM(C, 1, 1, 1); RUL(N, 1, 1, T);                            \
    MM(C, 1, 1, 2); RUH(N, 1, 1, T);                            \
    WM(13, C, 1, 1, 3); RY(N, 3, T);                            \
    MM(C, 1, 0, 3); RUL(N, 1, 0, T);                            \
    MM(C, 1, 0, 2); RUH(N, 1, 0, T);                            \
    MM(C, 1, 0, 0); RUL(N, 2, 0, T);                            \
    MM(C, 1, 0, 1); RUH(N, 2, 0, T);                            \
    WM(14, C, 2, 0, 1); RUL(N, 2, 1, T);                        \
    MM(C, 2, 0, 2); RUH(N, 2, 1, T);                            \
    MM(C, 2, 0, 3);                                             \
    MM(C, 2, 1, 3);                                             \
    if (UPD) { aw[0] += delta; aw[1] += delta; SB(); }          \
    MM(C, 2, 1, 2);                                             \
    if (UPD) { ax[0] += delta; ax[1] += delta; SB(); }          \
    MM(C, 2, 1, 1);                                             \
  } while (0)

struct Epi { f32x2 t[2], e[2]; u32x2 pk; };
constexpr float NLOG2E = -1.44269504088896340736f;
#define E_T(E, s, i)                                                                                             \
  do {                                                                                                           \
    E.t[0] = f32x2{acc[s][i][0], acc[s][i][1]} * f32x2{sc[i][0], sc[i][1]} + f32x2{sh[i][0], sh[i][1]}; SB();    \
    E.t[1] = f32x2{acc[s][i][2], acc[s][i][3]} * f32x2{sc[i][2], sc[i][3]} + f32x2{sh[i][2], sh[i][3]}; SB();    \
  } while (0)
#define E_X(E, r, h) do { E.e[r][h] = __builtin_amdgcn_exp2f(E.t[r][h]); SB(); } while (0)
#define E_D(E) do { E.e[0] = E.e[0] * NLOG2E + NLOG2E; SB(); E.e[1] = E.e[1] * NLOG2E + NLOG2E; SB(); } while (0)
#define E_R(E, r, h) do { E.e[r][h] = __builtin_amdgcn_rcpf(E.e[r][h]); SB(); } while (0)
#define E_O(E) do { E.t[0] = E.t[0] * E.e[0]; SB(); E.t[1] = E.t[1] * E.e[1]; SB(); } while (0)
#define E_P(E) do { E.pk[0] = pack_bf16(E.t[0][0], E.t[0][1]); SB(); E.pk[1] = pack_bf16(E.t[1][0], E.t[1][1]); SB(); } while (0)
#define E_W(E, s, i) do { asm volatile("ds_write_b64 %0, %1 offset:%2" ::"v"(astg[i]), "v"(E.pk), "n"((s) * 4096) : "memory"); SB(); } while (0)
#define E_HEAD(E, s, i) do { E_T(E, s, i); E_X(E, 0, 0); E_X(E, 0, 1); E_X(E, 1, 0); E_X(E, 1, 1); } while (0)
#define E_TAIL(E, s, i) do { E_D(E); E_R(E, 0, 0); E_R(E, 0, 1); E_R(E, 1, 0); E_R(E, 1, 1); E_O(E); E_P(E); E_W(E, s, i); } while (0)
#define NOP1() do { asm volatile("s_nop 1"); SB(); } while (0)

__device__ __forceinline__ void consume(char* shm, int wid, int lane) {
  const int nh = wid & 1, bh = wid >> 1;
  const int g = lane >> 4, n = lane & 15;
  const unsigned lbase = (unsigned)(size_t)(lds_char*)shm;
  unsigned ax[2], aw[2];
  ax[0] = lbase + bh * 2048 + n * 128 + ((g ^ ((n >> 1) & 7)) * 16);
  ax[1] = ax[0] ^ 64;
  const int q = n >> 2, p = lane & 3;
  aw[0] = lbase + (8 * g + q) * 128 + (((4 * nh + (p >> 1)) ^ (((q >> 1) & 1) << 2) ^ ((g & 1) << 1)) * 16) + 8 * (p & 1);
  aw[1] = aw[0] ^ 32;
  unsigned apar = lbase + PARAM_OFF + (32 * nh + 4 * g) * 4;
  unsigned astg[2];
#pragma unroll
  for (int i = 0; i < 2; ++i) astg[i] = lbase + bh * 2048 + n * 128 + (((4 * nh + 2 * i + (g >> 1)) ^ (n & 7)) * 16) + 8 * (g & 1);

  f32x4 acc[4][2] = {};
  Frags F0, F1;
  asm volatile("" : "+v"(acc[0][0]), "+v"(acc[0][1]), "+v"(acc[1][0]), "+v"(acc[1][1]), "+v"(acc[2][0]), "+v"(acc[2][1]),
               "+v"(acc[3][0]), "+v"(acc[3][1]), "+v"(ax[0]), "+v"(ax[1]), "+v"(aw[0]), "+v"(aw[1]), "+v"(apar), "+v"(astg[0]), "+v"(astg[1]));
  BARRIER();
  RUL(F0, 0, 0, 0); RUH(F0, 0, 0, 0); RY(F0, 0, 0); RY(F0, 1, 0); RY(F0, 2, 0); RUL(F0, 0, 1, 0); RUH(F0, 0, 1, 0);
  RUL(F0, 1, 1, 0); RUH(F0, 1, 1, 0); RY(F0, 3, 0); RUL(F0, 1, 0, 0); RUH(F0, 1, 0, 0);
  RUL(F0, 2, 0, 0); RUH(F0, 2, 0, 0); RUL(F0, 2, 1, 0); RUH(F0, 2, 1, 0);

#pragma unroll 1
  for (int st = 0; st < 6; ++st) {
    const int delta = (st == 3) ? -3 * STAGE_B : STAGE_B;
    STEP(F0, F1, 1, 1);
    STEP(F1, F0, 0, 0);
  }
  {
    const int delta = STAGE_B;
    STEP(F0, F1, 1, 1);
  }

  f32x4 sc[2], sh[2];
  Frags G;
#define RGY(s) RY(G, s, 1)
#define GM(k, i, s) \
  do { asm volatile("v_mfma_f32_16x16x32_bf16 %0, %1, %2, %0" : "+v"(acc[(s) - (k) + 1][i]) : "v"(CAT(F1.ul[k][i], F1.uh[k][i])), "v"(BC(G.y[s]))); SB(); } while (0)
#define WGM(n, k, i, s) \
  do { asm volatile("s_waitcnt lgkmcnt(%3)\n\tv_mfma_f32_16x16x32_bf16 %0, %1, %2, %0" : "+v"(acc[(s) - (k) + 1][i]) : "v"(CAT(F1.ul[k][i], F1.uh[k][i])), "v"(BC(G.y[s])), "n"(n)); SB(); } while (0)
  WM(13, F1, 0, 0, 0); BARRIER(); SB(); RUL(F0, 1, 0, 0);
  WM(13, F1, 0, 0, 1); RUH(F0, 1, 0, 0);
  WM(13, F1, 0, 0, 2); RY(F0, 0, 0);
  WM(7, F1, 1, 0, 0); RUL(F0, 2, 0, 0); RD128(sc[0], apar, 0); SB();
  MM(F1, 1, 0, 1); RUH(F0, 2, 0, 0); RD128(sh[0], apar, 256); SB();
  MM(F1, 1, 0, 2); RY(F0, 1, 0); RUL(F0, 0, 1, 0);
  MM(F1, 1, 0, 3); RGY(0);
  WM(10, F1, 2, 0, 1); RGY(1); RUH(F0, 0, 1, 0);
  MM(F1, 2, 0, 2); RUL(F1, 1, 0, 1);
  MM(F1, 2, 0, 3); RUH(F1, 1, 0, 1);
  MM(F1, 0, 1, 0); RUL(F0, 0, 0, 0);
  MM(F1, 0, 1, 1); RUH(F0, 0, 0, 0);
  MM(F1, 0, 1, 2); RUL(F1, 2, 0, 1);
  MM(F1, 1, 1, 0); RUH(F1, 2, 0, 1);
  MM(F1, 1, 1, 1); RY(F0, 2, 0);
  MM(F1, 1, 1, 2); RY(F0, 3, 0);
  MM(F1, 1, 1, 3); RUL(F1, 0, 0, 1);
  MM(F1, 2, 1, 1); RUH(F1, 0, 0, 1);
  MM(F1, 2, 1, 2); RGY(2);
  MM(F1, 2, 1, 3); RGY(3);

  Epi e0, e1, e2, e3;
  WM(10, F0, 1, 0, 0); RUL(F0, 1, 1, 0); RUH(F0, 1, 1, 0);
  MM(F0, 2, 0, 1); RUL(F0, 2, 1, 0); RUH(F0, 2, 1, 0);
  GM(1, 0, 0); RUL(F1, 0, 1, 1);
  WGM(9, 2, 0, 1); RUH(F1, 0, 1, 1); RUL(F1, 1, 1, 1);
  MM(F0, 0, 0, 2); RUH(F1, 1, 1, 1); RUL(F1, 2, 1, 1);
  MM(F0, 1, 0, 3); RUH(F1, 2, 1, 1);
  WGM(10, 0, 0, 2);
  asm volatile("" : "+v"(sc[0]), "+v"(sh[0]), "+v"(acc[0][0])); SB();
  E_T(e0, 0, 0);
  GM(1, 0, 3); E_X(e0, 0, 0);
  MM(F0, 0, 0, 0); E_X(e0, 0, 1);
  MM(F0, 1, 0, 1); E_X(e0, 1, 0);
  MM(F0, 2, 0, 2); E_X(e0, 1, 1);
  GM(0, 0, 0); E_D(e0);
  GM(1, 0, 1); E_R(e0, 0, 0);
  GM(2, 0, 2); E_R(e0, 0, 1);
  MM(F0, 0, 0, 1); E_R(e0, 1, 0);
  MM(F0, 1, 0, 2); E_R(e0, 1, 1);
  MM(F0, 2, 0, 3); E_O(e0);
  GM(0, 0, 1); E_P(e0);
  GM(1, 0, 2); E_W(e0, 0, 0);
  asm volatile("s_waitcnt lgkmcnt(1)" : "+v"(acc[3][0])); SB();
  GM(2, 0, 3); E_T(e1, 3, 0);
  MM(F0, 1, 1, 0); E_X(e1, 0, 0); RD128(sc[1], apar, 64); SB();
  MM(F0, 2, 1, 1); E_X(e1, 0, 1); RD128(sh[1], apar, 320); SB();
  GM(1, 1, 0); E_X(e1, 1, 0);
  GM(2, 1, 1); E_X(e1, 1, 1);
  MM(F0, 0, 1, 2); E_D(e1);
  MM(F0, 1, 1, 3); E_R(e1, 0, 0);
  GM(0, 1, 2); E_R(e1, 0, 1);
  GM(1, 1, 3); E_R(e1, 1, 0);
  MM(F0, 0, 1, 0); E_R(e1, 1, 1);
  MM(F0, 1, 1, 1); E_O(e1);
  MM(F0, 2, 1, 2); E_P(e1);
  GM(0, 1, 0); E_W(e1, 3, 0);
  asm volatile("s_waitcnt lgkmcnt(1)" : "+v"(sc[1]), "+v"(sh[1]), "+v"(acc[0][1])); SB();
  GM(1, 1, 1); E_T(e2, 0, 1);
  GM(2, 1, 2); E_X(e2, 0, 0);
  MM(F0, 0, 1, 1); E_X(e2, 0, 1);
  MM(F0, 1, 1, 2); E_X(e2, 1, 0);
  MM(F0, 2, 1, 3); E_X(e2, 1, 1);
  GM(0, 1, 1); E_D(e2);
  GM(1, 1, 2); E_R(e2, 0, 0);
  GM(2, 1, 3);
  asm volatile("" : "+v"(acc[3][1])); SB();
  E_R(e2, 0, 1); E_R(e2, 1, 0); E_R(e2, 1, 1);
  E_T(e3, 3, 1);
  E_O(e2); E_P(e2); E_W(e2, 0, 1);
  E_X(e3, 0, 0); E_X(e3, 0, 1); E_X(e3, 1, 0); E_X(e3, 1, 1);
  asm volatile("" : "+v"(acc[1][0]), "+v"(acc[2][0]), "+v"(acc[1][1]), "+v"(acc[2][1])); SB();
  E_T(e0, 1, 0);
  E_TAIL(e3, 3, 1);
  E_X(e0, 0, 0); E_X(e0, 0, 1); E_X(e0, 1, 0); E_X(e0, 1, 1);
  E_T(e1, 2, 0);
  E_D(e0); E_R(e0, 0, 0); E_R(e0, 0, 1); E_R(e0, 1, 0); E_R(e0, 1, 1);
  WAIT_L0();
  BARRIER();
  SB();
  E_O(e0); E_P(e0); E_W(e0, 1, 0);
  E_X(e1, 0, 0); E_X(e1, 0, 1); E_X(e1, 1, 0); E_X(e1, 1, 1);
  E_T(e2, 1, 1);
  E_TAIL(e1, 2, 0);
  E_X(e2, 0, 0); E_X(e2, 0, 1); E_X(e2, 1, 0); E_X(e2, 1, 1);
  E_T(e3, 2, 1);
  E_TAIL(e2, 1, 1);
  E_X(e3, 0, 0); E_X(e3, 0, 1); E_X(e3, 1, 0); E_X(e3, 1, 1);
  E_TAIL(e3, 2, 1);
  WAIT_L0();
  BARRIER();
}

__global__ __launch_bounds__(512) void conv_bn_silu(
    const char* __restrict__ x, const char* __restrict__ w, const unsigned short* __restrict__ mean,
    const unsigned short* __restrict__ var, const unsigned short* __restrict__ g,
    const unsigned short* __restrict__ bb, char* __restrict__ out) {
  __shared__ __attribute__((aligned(1024))) char shm[NSLOT * STAGE_B];
  const int tid = threadIdx.x;
  const int wid = __builtin_amdgcn_readfirstlane(tid >> 6);
  const int lane = tid & 63;

  if (wid < 4) {
    consume(shm, wid, lane);
    return;
  }

  const int xcd = blockIdx.x & 7, idx = blockIdx.x >> 3;
  const int mt = (xcd >> 1) * 8 + (idx & 7);
  const int nt = (xcd & 1) * 4 + (idx >> 3);
  const int b0 = mt * 32, n0 = nt * 64;

  const int rot = 6 * ((blockIdx.x >> 0) & 1) + 2 * ((blockIdx.x >> 1) & 1) + 3 * ((blockIdx.x >> 2) & 1) + 4 * ((blockIdx.x >> 4) & 1) + 3 * ((blockIdx.x >> 5) & 1) + 5 * ((blockIdx.x >> 6) & 1);
  const int lw = wid - 4;
  const __amdgpu_buffer_rsrc_t rx = __builtin_amdgcn_make_buffer_rsrc((void*)x, 0, 1024 * 4 * 512 * 2, SRD3);
  const __amdgpu_buffer_rsrc_t rw = __builtin_amdgcn_make_buffer_rsrc((void*)w, 0, 3 * 512 * 512 * 2, SRD3);
  const int lr = lane >> 3, ls = lane & 7;
  const int vx0 = lr * 4096 + ((ls ^ (lane >> 4)) * 16);
  const int vx1 = lr * 4096 + ((ls ^ (4 + (lane >> 4))) * 16);
  const int vw0 = lr * 1024 + ((ls ^ (((lane >> 4) & 1) << 2)) * 16);
  const int vw1 = vw0 ^ 32;
  const int sx = (b0 * 4 + lw) * 1024;
  auto stage = [&](int st, int slotoff) {
    const int cs = (st + rot) & 7;
    lds_char* slot = (lds_char*)shm + slotoff;
#pragma unroll
    for (int j = 0; j < 4; ++j)
      __builtin_amdgcn_raw_ptr_buffer_load_lds(rx, (lds_void*)(slot + (lw * 4 + j) * 1024), 16, (j & 1) ? vx1 : vx0,
                                               sx + j * 8 * 4096 + cs * 128, 0, LDAUX);
#pragma unroll
    for (int j = 0; j < 6; ++j) {
      const int r = lw * 48 + j * 8;
      const int k = r >> 6, cc = r & 63;
      __builtin_amdgcn_raw_ptr_buffer_load_lds(rw, (lds_void*)(slot + XB + (lw * 6 + j) * 1024), 16, (j & 1) ? vw1 : vw0,
                                               (k * 512 + cs * 64 + cc) * 1024 + n0 * 2, 0, LDAUX);
    }
  };
  const unsigned short pm = mean[n0 + lane], pv = var[n0 + lane], pg = g[n0 + lane], pb = bb[n0 + lane];
  SB();
  stage(0, 0);
  stage(1, STAGE_B);
  WAIT_V(10);
  BARRIER();
  stage(2, 2 * STAGE_B);
  const float scale0 = bf2f(pg) * __builtin_amdgcn_rsqf(bf2f(pv) + 1e-5f);
  const float scale = scale0 * -1.44269504088896340736f;
  const float shift = (bf2f(pb) - bf2f(pm) * scale0) * -1.44269504088896340736f;
  int slotoff = 3 * STAGE_B;
#pragma unroll 1
  for (int k = 1; k <= 5; ++k) {
    WAIT_V(10);
    BARRIER();
    stage(k + 2, slotoff);
    slotoff = (slotoff == 3 * STAGE_B) ? 0 : slotoff + STAGE_B;
  }
  WAIT_V(10);
  BARRIER();
  if (lw == 0) {
    const unsigned a = (unsigned)(size_t)(lds_char*)shm + PARAM_OFF + lane * 4;
    asm volatile("ds_write_b32 %0, %1\n\tds_write_b32 %0, %2 offset:256" ::"v"(a), "v"(scale), "v"(shift) : "memory");
  }
  WAIT_V(0);
  WAIT_L0();
  BARRIER();

  const __amdgpu_buffer_rsrc_t ro = __builtin_amdgcn_make_buffer_rsrc((void*)out, 0, 1024 * 4 * 512 * 2, SRD3);
  const unsigned sbase = (unsigned)(size_t)(lds_char*)shm + (lw * 8 + lr) * 128 + ((ls ^ lr) * 16);
  const int vo = (b0 + lw * 8 + lr) * 4096 + n0 * 2 + ls * 16;
#pragma unroll
  for (int ph = 0; ph < 2; ++ph) {
    BARRIER();
    const int sa = ph ? 1 : 0, sb = ph ? 2 : 3;
    u32x4 va, vb;
    asm volatile("ds_read_b128 %0, %2 offset:%3\n\tds_read_b128 %1, %2 offset:%4\n\ts_waitcnt lgkmcnt(0)"
                 : "=&v"(va), "=&v"(vb) : "v"(sbase), "n"(sa * 4096), "n"(sb * 4096) : "memory");
    __builtin_amdgcn_raw_buffer_store_b128(va, ro, vo, sa * 1024, STAUX);
    __builtin_amdgcn_raw_buffer_store_b128(vb, ro, vo, sb * 1024, STAUX);
  }
}

extern "C" void kernel_launch(void* const* d_in, const int* in_sizes, int n_in, void* d_out, int out_size,
                              void* d_ws, size_t ws_size, hipStream_t stream) {
  conv_bn_silu<<<256, 512, 0, stream>>>((const char*)d_in[0], (const char*)d_in[1], (const unsigned short*)d_in[2],
                                        (const unsigned short*)d_in[3], (const unsigned short*)d_in[4],
                                        (const unsigned short*)d_in[5], (char*)d_out);
}
```
